```python
import torch

def _softmax(x, axis=-1):
    return torch.softmax(x, dim=axis)
def _gelu(x):
    return torch.nn.functional.gelu(x, approximate="tanh")
def _silu(x):
    return torch.nn.functional.silu(x)
def _sigmoid(x):
    return torch.sigmoid(x)
def _layernorm(x, axis=-1, eps=1e-5):
    m = x.mean(dim=axis, keepdim=True)
    v = x.var(dim=axis, keepdim=True, unbiased=False)
    return (x - m) / torch.sqrt(v + eps)
def _scatter_add(tgt, idx, src, axis=0):
    return tgt.clone().index_add_(axis, idx, src)
def _segment_sum(src, seg, n):
    out = torch.zeros((n,) + src.shape[1:], dtype=src.dtype)
    return out.index_add_(0, seg, src)
def _pad(x, pw):
    flat = tuple(v for pair in reversed(pw) for v in pair)
    return torch.nn.functional.pad(x, flat)

kernel_name = 'parallel_sum__softmax_full_mha__glu_gated_reglu__prompt'

def setup_inputs():

    _w = (lambda a: (lambda t: t.to(torch.bfloat16) if t.is_floating_point() else t)(torch.as_tensor(a)))
    def _A_setup():
        B, S, D = 1, 2048, 640
        return {
            "x": _w((lambda *s: torch.randn(*s, dtype=torch.bfloat16) * 0.05)(B, S, D)),
            "wqkv": _w((lambda *s: torch.randn(*s, dtype=torch.bfloat16) * 0.05)(D, 3 * D)),
            "wo": _w((lambda *s: torch.randn(*s, dtype=torch.bfloat16) * 0.05)(D, D)),
        }
    _A_in = _A_setup()

    def _B_setup():
        B, S, D, F = 1, 2048, 640, 1536
        return {
            "x": _w((lambda *s: torch.randn(*s, dtype=torch.bfloat16) * 0.05)(B, S, D)),
            "W_gate": _w((lambda *s: torch.randn(*s, dtype=torch.bfloat16) * 0.4)(D, F) * (2.0 / D) ** 0.5),
            "W_up": _w((lambda *s: torch.randn(*s, dtype=torch.bfloat16) * 0.4)(D, F) * (2.0 / D) ** 0.5),
            "W_down": _w((lambda *s: torch.randn(*s, dtype=torch.bfloat16) * 0.4)(F, D) * (1.0 / F) ** 0.5),
        }
    _B_in = _B_setup()

    return {
        "A_x": _A_in["x"],
        "A_wqkv": _A_in["wqkv"],
        "A_wo": _A_in["wo"],
        "B_x": _B_in["x"],
        "B_W_gate": _B_in["W_gate"],
        "B_W_up": _B_in["W_up"],
        "B_W_down": _B_in["W_down"],
    }

def reference(A_x, A_wqkv, A_wo, B_x, B_W_gate, B_W_up, B_W_down):
    def _A_ref(x, wqkv, wo):
        B, S, D = x.shape
        H = 32; Dh = D // H
        qkv = (lambda x, *d: x.permute(*d))((x @ wqkv).reshape(B, S, 3, H, Dh), 2, 0, 3, 1, 4)
        q, k, v = qkv[0], qkv[1], qkv[2]
        scale = Dh ** -0.5
        scores = torch.einsum("bhqd,bhkd->bhqk", q, k) * scale
        p = _softmax(scores, axis=-1)
        o = torch.einsum("bhqk,bhkd->bhqd", p, v)
        return (lambda x, *d: x.permute(*d))(o, 0, 2, 1, 3).reshape(B, S, D) @ wo

    def _B_ref(x, W_gate, W_up, W_down):
        gate = torch.einsum("bsd,df->bsf", x, W_gate)
        up = torch.einsum("bsd,df->bsf", x, W_up)
        gate = gate * (lambda x, dt: x.to(dt))(gate > 0, gate.dtype)
        h = gate * up
        return torch.einsum("bsf,fd->bsd", h, W_down)

    x = A_x
    xn = _layernorm(x, axis=-1)
    return x + _A_ref(xn, A_wqkv, A_wo) + _B_ref(xn, B_W_gate, B_W_up, B_W_down)
```

```cpp
#include <hip/hip_runtime.h>
#include <hip/hip_bf16.h>
#include <stdint.h>

typedef short bf16x8 __attribute__((ext_vector_type(8)));
typedef short bf16x4 __attribute__((ext_vector_type(4)));
typedef float f32x16 __attribute__((ext_vector_type(16)));
typedef float f32x4 __attribute__((ext_vector_type(4)));
typedef float f32x2 __attribute__((ext_vector_type(2)));
typedef unsigned u32x4 __attribute__((ext_vector_type(4)));
typedef unsigned u32x2 __attribute__((ext_vector_type(2)));
typedef __bf16 bf16v2 __attribute__((ext_vector_type(2)));
#define LAS __attribute__((address_space(3)))
#define GAS __attribute__((address_space(1)))
#define DI __device__ __forceinline__
typedef LAS char* ldsp;
typedef const GAS char* gcp;
typedef GAS char* gp;

constexpr int S = 2048, D = 640, NH = 32, DH = 20, FF = 1536;
constexpr size_t QK_TILE = 1280, V_TILE = 1344;
constexpr size_t WS_QF = 0;
constexpr size_t WS_KF = WS_QF + (size_t)NH * 64 * QK_TILE;
constexpr size_t WS_VF = WS_KF + (size_t)NH * 64 * QK_TILE;
constexpr size_t WS_XN = WS_VF + (size_t)NH * 64 * V_TILE;
constexpr size_t WS_HF = WS_XN + (size_t)S * D * 2;
constexpr size_t WS_OF = WS_HF + (size_t)S * FF * 2;
constexpr size_t WS_Y = WS_OF + (size_t)S * D * 2;
constexpr size_t WS_FLAG = WS_Y + (size_t)S * D * 2;
constexpr float QSCALE = 0.22360679774997896f * 1.4426950408889634f;

#define MFMA32(a, b, c) __builtin_amdgcn_mfma_f32_32x32x16_bf16((a), (b), (c), 0, 0, 0)
#define MFMA32K8(a, b, c) __builtin_amdgcn_mfma_f32_32x32x8bf16_1k((a), (b), (c), 0, 0, 0)
#define MFMA16(a, b, c) __builtin_amdgcn_mfma_f32_16x16x32_bf16((a), (b), (c), 0, 0, 0)
#define SBAR() __builtin_amdgcn_sched_barrier(0)

DI unsigned cvtpk(float lo, float hi) { f32x2 f = {lo, hi}; return __builtin_bit_cast(unsigned, __builtin_convertvector(f, bf16v2)); }
DI float bf_lo(unsigned u) { return __uint_as_float(u << 16); }
DI float bf_hi(unsigned u) { return __uint_as_float(u & 0xffff0000u); }
DI float rbf(float x) { return bf_lo(cvtpk(x, x)); }
DI float dot2(unsigned a, unsigned b, float c) { return __builtin_amdgcn_fdot2_f32_bf16(__builtin_bit_cast(bf16v2, a), __builtin_bit_cast(bf16v2, b), c, false); }
DI float xhalf(float v) {
    auto rr = __builtin_amdgcn_permlane32_swap(__float_as_uint(v), __float_as_uint(v), false, false);
    return __uint_as_float((threadIdx.x & 32) ? rr[0] : rr[1]);
}
DI void glds16(unsigned voff, const void* sbase, unsigned lds_dst) {
    asm volatile("s_mov_b32 m0, %2\n\ts_nop 0\n\tglobal_load_lds_dwordx4 %0, %1" ::"v"(voff), "s"(sbase), "s"(lds_dst) : "memory");
}
DI void gld16(bf16x8& dst, unsigned voff, const void* sbase) {
    asm volatile("global_load_dwordx4 %0, %1, %2" : "=v"(dst) : "v"(voff), "s"(sbase) : "memory");
}
#define WAITV(n) asm volatile("s_waitcnt vmcnt(" #n ")" ::: "memory")
DI bf16x8 lds128(ldsp p) { return *(const LAS bf16x8*)p; }
DI bf16x4 ldstr(ldsp p) { return __builtin_amdgcn_ds_read_tr16_b64_v4i16((LAS bf16x4*)p); }
DI bf16x8 cat(bf16x4 a, bf16x4 b) { return __builtin_shufflevector(a, b, 0, 1, 2, 3, 4, 5, 6, 7); }
DI bf16x8 pack8(const f32x16& x, int s) {
    u32x4 p = {cvtpk(x[8 * s], x[8 * s + 1]), cvtpk(x[8 * s + 2], x[8 * s + 3]), cvtpk(x[8 * s + 4], x[8 * s + 5]), cvtpk(x[8 * s + 6], x[8 * s + 7])};
    return __builtin_bit_cast(bf16x8, p);
}

constexpr int K1_SLOT = 32768, K1_NS = 4, K1_STAT = K1_SLOT * K1_NS, K1_LDS = K1_STAT + 4096;

template <bool ISV>
DI void k1_body(const char* x, const char* w, char* ws, ldsp lds, int rb, int cb) {
    const int tid = threadIdx.x, lane = tid & 63, r = lane & 31, hh = lane >> 5;
    const int wid = __builtin_amdgcn_readfirstlane(tid >> 6), wm = wid >> 1, wn = wid & 1;
    const unsigned lds0 = (unsigned)(uintptr_t)lds;
    u32x4 xraw[4];
    if (!ISV) {
#pragma unroll
        for (int i = 0; i < 4; ++i) xraw[i] = *(const GAS u32x4*)((gcp)x + (size_t)(rb * 128 + wid * 32 + r) * (D * 2) + (cb * 64 + i * 16 + hh * 8) * 2);
    }
    const unsigned voffA0 = (lane >> 3) * (D * 2) + (((lane & 7) ^ ((lane >> 4) & 7)) << 4);
    const unsigned voffA1 = (lane >> 3) * (D * 2) + (((lane & 7) ^ ((4 + (lane >> 4)) & 7)) << 4);
    const unsigned voffB = (lane >> 4) * (3 * D * 2) + (((lane & 15) ^ ((lane >> 4) << 2)) << 4);
    const char* baseA = x + (size_t)(rb * 128 + wid * 32) * (D * 2);
    const char* baseB = w + (size_t)(wid * 16) * (3 * D * 2) + cb * 256;
    auto stage = [&](int s) {
        const unsigned dst = lds0 + (s % K1_NS) * K1_SLOT + wid * 4096;
#pragma unroll
        for (int i = 0; i < 4; ++i) glds16((i & 1) ? voffA1 : voffA0, baseA + i * 8 * (D * 2) + s * 128, dst + i * 1024);
#pragma unroll
        for (int i = 0; i < 4; ++i) glds16(voffB, baseB + (size_t)(s * 64 + i * 4) * (3 * D * 2), dst + 16384 + i * 1024);
    };
    stage(0); stage(1); stage(2);
    const int ta0 = wm * 2 + wn, ta1 = wm * 2 + (wn ^ 1);
    const int tb0 = wn * 2 + wm, tb1 = wn * 2 + (wm ^ 1);
    ldsp pa[4];
#pragma unroll
    for (int ks = 0; ks < 4; ++ks) pa[ks] = lds + r * 128 + (((2 * ks + hh) ^ ((r >> 1) & 7)) << 4);
    const int blk = (lane >> 4) & 1, q = (lane & 15) >> 2, p = lane & 3;
    ldsp pb0 = lds + 16384 + (8 * hh + q) * 256 + (((4 * tb0 + 2 * blk + (p >> 1)) ^ (q << 2)) << 4) + 8 * (p & 1);
    ldsp pb1 = lds + 16384 + (8 * hh + q) * 256 + (((4 * tb1 + 2 * blk + (p >> 1)) ^ (q << 2)) << 4) + 8 * (p & 1);
    f32x16 acc00 = {}, acc01 = {}, acc10 = {}, acc11 = {};
    float sx = 0.f, sxx = 0.f, sw = 0.f;
#pragma unroll
    for (int s = 0; s < 10; ++s) {
        if (s < 8) asm volatile("s_waitcnt vmcnt(16) lgkmcnt(0)\n\ts_barrier" ::: "memory");
        else if (s == 8) asm volatile("s_waitcnt vmcnt(8) lgkmcnt(0)\n\ts_barrier" ::: "memory");
        else asm volatile("s_waitcnt vmcnt(0) lgkmcnt(0)\n\ts_barrier" ::: "memory");
        if (s + 3 < 10) stage(s + 3);
        const int so = (s % K1_NS) * K1_SLOT;
#pragma unroll
        for (int ks = 0; ks < 4; ++ks) {
            const bf16x8 a0 = lds128(pa[ks] + so + ta0 * 4096), a1 = lds128(pa[ks] + so + ta1 * 4096);
            const bf16x8 b0 = cat(ldstr(pb0 + so + ks * 4096), ldstr(pb0 + so + ks * 4096 + 1024));
            const bf16x8 b1 = cat(ldstr(pb1 + so + ks * 4096), ldstr(pb1 + so + ks * 4096 + 1024));
            if (ISV) { acc00 = MFMA32(a0, b0, acc00); acc01 = MFMA32(a0, b1, acc01); acc10 = MFMA32(a1, b0, acc10); acc11 = MFMA32(a1, b1, acc11); }
            else     { acc00 = MFMA32(b0, a0, acc00); acc01 = MFMA32(b1, a0, acc01); acc10 = MFMA32(b0, a1, acc10); acc11 = MFMA32(b1, a1, acc11); }
            const u32x4 au = __builtin_bit_cast(u32x4, a0), bu = __builtin_bit_cast(u32x4, b0);
#pragma unroll
            for (int i = 0; i < 4; ++i) { sx = dot2(au[i], 0x3F803F80u, sx); sxx = dot2(au[i], au[i], sxx); sw = dot2(bu[i], 0x3F803F80u, sw); }
        }
    }
    sx += xhalf(sx); sxx += xhalf(sxx); sw += xhalf(sw);
    {
        const float mean = sx * (1.f / D), var = fmaxf(sxx * (1.f / D) - mean * mean, 0.f);
        const float mb = rbf(mean), den = rbf(sqrtf(rbf(rbf(var) + 1e-5f))), rs = 1.f / den;
        LAS float* st = (LAS float*)(lds + K1_STAT);
        if (hh == 0) { st[ta0 * 32 + r] = rs; st[128 + ta0 * 32 + r] = -mb * rs; st[384 + ta0 * 32 + r] = mb; st[512 + ta0 * 32 + r] = den; }
        else st[256 + tb0 * 32 + r] = sw;
    }
    __syncthreads();
    const LAS float* st = (const LAS float*)(lds + K1_STAT);
    if (!ISV) {
        const bool isq = cb < 5; const int cbs = isq ? cb : cb - 5;
        gp dstb = (gp)ws + (isq ? WS_QF : WS_KF);
        const float sc = isq ? QSCALE : 1.f;
#pragma unroll
        for (int i = 0; i < 2; ++i) {
            const int ta = i ? ta1 : ta0;
            const float ca = st[ta * 32 + r] * sc, cbv = st[128 + ta * 32 + r] * sc;
#pragma unroll
            for (int j = 0; j < 2; ++j) {
                const int tb = j ? tb1 : tb0;
                const f32x16& ac = i ? (j ? acc11 : acc10) : (j ? acc01 : acc00);
#pragma unroll
                for (int g = 0; g < 4; ++g) {
                    const f32x4 cs = *(const LAS f32x4*)(st + 256 + tb * 32 + 8 * g + 4 * hh);
                    u32x2 o;
                    o[0] = cvtpk(fmaf(ac[4 * g], ca, cs[0] * cbv), fmaf(ac[4 * g + 1], ca, cs[1] * cbv));
                    o[1] = cvtpk(fmaf(ac[4 * g + 2], ca, cs[2] * cbv), fmaf(ac[4 * g + 3], ca, cs[3] * cbv));
                    const int u0 = cbs * 32 + tb * 8 + 2 * g, u1 = u0 + 1;
                    const unsigned o0 = (u0 / 5) * (64 * QK_TILE) + (u0 % 5) * 256, o1 = (u1 / 5) * (64 * QK_TILE) + (u1 % 5) * 256;
                    *(GAS u32x2*)(dstb + (hh ? o1 : o0) + (rb * 4 + ta) * QK_TILE + r * 8) = o;
                }
            }
        }
        const float mb = st[384 + wid * 32 + r], den = st[512 + wid * 32 + r];
#pragma unroll
        for (int i = 0; i < 4; ++i) {
            u32x4 o;
#pragma unroll
            for (int e = 0; e < 4; ++e) o[e] = cvtpk(rbf(bf_lo(xraw[i][e]) - mb) / den, rbf(bf_hi(xraw[i][e]) - mb) / den);
            *(GAS u32x4*)((gp)ws + WS_XN + (size_t)((rb * 4 + wid) * 40 + cb * 4 + i) * 1024 + lane * 16) = o;
        }
    } else {
        const int cbs = cb - 10;
#pragma unroll
        for (int j = 0; j < 2; ++j) {
            const int tb = j ? tb1 : tb0;
            const int n = cbs * 128 + tb * 32 + r, head = n / DH, d = n - head * DH;
            const float cs = st[256 + tb * 32 + r];
            gp dst = (gp)ws + WS_VF + (size_t)head * (64 * V_TILE) + hh * 336 + d * 16;
#pragma unroll
            for (int i = 0; i < 2; ++i) {
                const int ta = i ? ta1 : ta0;
                const f32x16& ac = i ? (j ? acc11 : acc10) : (j ? acc01 : acc00);
                f32x16 v;
#pragma unroll
                for (int g = 0; g < 4; ++g) {
                    const f32x4 ca = *(const LAS f32x4*)(st + ta * 32 + 8 * g + 4 * hh), cbv = *(const LAS f32x4*)(st + 128 + ta * 32 + 8 * g + 4 * hh);
#pragma unroll
                    for (int e = 0; e < 4; ++e) v[4 * g + e] = fmaf(ac[4 * g + e], ca[e], cs * cbv[e]);
                }
                *(GAS bf16x8*)(dst + (rb * 4 + ta) * V_TILE) = pack8(v, 0);
                *(GAS bf16x8*)(dst + (rb * 4 + ta) * V_TILE + 672) = pack8(v, 1);
            }
        }
    }
}

__global__ __launch_bounds__(256, 2) void k1_qkv(const char* __restrict__ x, const char* __restrict__ w, char* __restrict__ ws) {
    extern __shared__ __attribute__((aligned(16))) char smem[];
    const int bid = blockIdx.x;
    if (bid >= 240) {
        const int t = (bid - 240) * 256 + threadIdx.x;
        const u32x4 ones = {0x3F803F80u, 0x3F803F80u, 0x3F803F80u, 0x3F803F80u};
        *(GAS u32x4*)((gp)ws + WS_VF + (size_t)(2 * t) * 336 + 320) = ones;
        *(GAS u32x4*)((gp)ws + WS_VF + (size_t)(2 * t + 1) * 336 + 320) = ones;
        if (t < 256) *(GAS unsigned*)((gp)ws + WS_FLAG + t * 128) = 0u;
        return;
    }
    const int xcd = bid & 7, t = (xcd & 1) * 30 + (bid >> 3), cb = t >> 2, rb = (xcd >> 1) * 4 + (t & 3);
    if (cb >= 10) k1_body<true>(x, w, ws, (ldsp)smem, rb, cb);
    else k1_body<false>(x, w, ws, (ldsp)smem, rb, cb);
}

struct KV { bf16x8 ka; bf16x4 kb; bf16x8 v0, v1; };
DI void load_k(KV& f, gcp kt, unsigned lane8, unsigned lane4) {
    const bf16x4 lo = *(const GAS bf16x4*)(kt + lane8), hi = *(const GAS bf16x4*)(kt + 512 + lane8);
    f.ka = cat(lo, hi);
    const u32x2 kb = {*(const GAS unsigned*)(kt + 1024 + lane4), 0x00003F80u};
    f.kb = __builtin_bit_cast(bf16x4, kb);
}
DI void load_v(KV& f, gcp vt, unsigned voff) { f.v0 = *(const GAS bf16x8*)(vt + voff); f.v1 = *(const GAS bf16x8*)(vt + 672 + voff); }
DI f32x16 qk(const KV& f, const bf16x8& qa, const bf16x4& qb) { f32x16 z = {}; return MFMA32K8(f.kb, qb, MFMA32(f.ka, qa, z)); }
DI float max16(const f32x16& s) {
    float a = fmaxf(fmaxf(s[0], s[1]), s[2]), b = fmaxf(fmaxf(s[3], s[4]), s[5]);
    a = fmaxf(fmaxf(a, s[6]), s[7]); b = fmaxf(fmaxf(b, s[8]), s[9]); a = fmaxf(fmaxf(a, s[10]), s[11]); b = fmaxf(fmaxf(b, s[12]), s[13]);
    a = fmaxf(fmaxf(a, s[14]), s[15]); a = fmaxf(a, b);
    return fmaxf(a, xhalf(a));
}
constexpr int ATT_LDS = 8 * 9216;

template <class BarT>
DI void attn_body(char* ws, ldsp lds, int head, int qblk, int aw, BarT bar) {
    const int lane = threadIdx.x & 63, r = lane & 31, hh = lane >> 5;
    const int qh = aw & 1, kq = aw >> 1;
    const unsigned lane8 = lane * 8, lane4 = r * 8 + hh * 4, voff = hh * 336 + (r < 20 ? r : 20) * 16;
    gcp qbase = (gcp)ws + WS_QF + (size_t)(head * 64 + qblk * 8 + qh * 4) * QK_TILE;
    gcp kbase = (gcp)ws + WS_KF + (size_t)(head * 64) * QK_TILE;
    gcp vbase = (gcp)ws + WS_VF + (size_t)(head * 64) * V_TILE;
    bf16x8 qa[4]; bf16x4 qb[4];
    KV f0, f1;
    load_k(f1, kbase, lane8, lane4);
#pragma unroll
    for (int i = 0; i < 4; ++i) {
        gcp qt = qbase + ((i + kq) & 3) * QK_TILE;
        qa[i] = cat(*(const GAS bf16x4*)(qt + lane8), *(const GAS bf16x4*)(qt + 512 + lane8));
        const u32x2 t = {*(const GAS unsigned*)(qt + 1024 + lane4), 0u};
        qb[i] = __builtin_bit_cast(bf16x4, t);
    }
    gcp kt = kbase + (size_t)(kq * 16) * QK_TILE; gcp vt = vbase + (size_t)(kq * 16) * V_TILE;
    load_k(f0, kt, lane8, lane4); load_v(f0, vt, voff);
#pragma unroll
    for (int i = 0; i < 4; ++i) {
        const float m0 = max16(qk(f1, qa[i], qb[i]));
        u32x2 t = __builtin_bit_cast(u32x2, qb[i]);
        t[1] = hh ? 0u : (cvtpk(-m0, 0.f) & 0xffffu);
        qb[i] = __builtin_bit_cast(bf16x4, t);
    }
    f32x16 o[4] = {};
    f32x16 sc = qk(f0, qa[0], qb[0]);
#define ATT_STEP(CUR, NXT, i)                                                                   \
    {                                                                                           \
        f32x16 sn = qk((i) < 3 ? CUR : NXT, qa[((i) + 1) & 3], qb[((i) + 1) & 3]);              \
        _Pragma("unroll") for (int e = 0; e < 16; ++e) sc[e] = __builtin_amdgcn_exp2f(sc[e]);   \
        o[i] = MFMA32(CUR.v0, pack8(sc, 0), o[i]);                                              \
        o[i] = MFMA32(CUR.v1, pack8(sc, 1), o[i]);                                              \
        sc = sn;                                                                                \
    }
    for (int t = 0; t < 16; t += 2) {
        load_k(f1, kt + QK_TILE, lane8, lane4); load_v(f1, vt + V_TILE, voff);
        ATT_STEP(f0, f1, 0) ATT_STEP(f0, f1, 1) ATT_STEP(f0, f1, 2) ATT_STEP(f0, f1, 3)
        const int adv = (t + 2 < 16) ? 2 : 0;
        kt += adv * QK_TILE; vt += adv * V_TILE;
        load_k(f0, kt, lane8, lane4); load_v(f0, vt, voff);
        ATT_STEP(f1, f0, 0) ATT_STEP(f1, f0, 1) ATT_STEP(f1, f0, 2) ATT_STEP(f1, f0, 3)
    }
#pragma unroll
    for (int i = 1; i < 4; ++i) {
        ldsp d = lds + (((i + kq) & 3) * 2 + qh) * 9216 + (i - 1) * 3072 + lane * 16;
#pragma unroll
        for (int g = 0; g < 3; ++g) *(LAS f32x4*)(d + g * 1024) = f32x4{o[i][4 * g], o[i][4 * g + 1], o[i][4 * g + 2], o[i][4 * g + 3]};
    }
    f32x4 own[3];
#pragma unroll
    for (int g = 0; g < 3; ++g) own[g] = f32x4{o[0][4 * g], o[0][4 * g + 1], o[0][4 * g + 2], o[0][4 * g + 3]};
    bar();
#pragma unroll
    for (int sl = 0; sl < 3; ++sl)
#pragma unroll
        for (int g = 0; g < 3; ++g) own[g] += *(const LAS f32x4*)(lds + aw * 9216 + sl * 3072 + g * 1024 + lane * 16);
    float chk = 0.f;
#pragma unroll
    for (int g = 0; g < 3; ++g) chk += fabsf(own[g][0]) + fabsf(own[g][1]) + fabsf(own[g][2]) + fabsf(own[g][3]);
    const int qt_idx = qblk * 8 + qh * 4 + kq;
    if (__builtin_expect(__any(!(chk < 3.0e38f)), 0)) {
        gcp qt = (gcp)ws + WS_QF + (size_t)(head * 64 + qt_idx) * QK_TILE;
        const bf16x8 qa1 = cat(*(const GAS bf16x4*)(qt + lane8), *(const GAS bf16x4*)(qt + 512 + lane8));
        const u32x2 tq = {*(const GAS unsigned*)(qt + 1024 + lane4), 0u};
        const bf16x4 qb1 = __builtin_bit_cast(bf16x4, tq);
        f32x16 oo = {}; float m = -INFINITY;
        for (int t = 0; t < 64; ++t) {
            KV f; load_k(f, kbase + (size_t)t * QK_TILE, lane8, lane4); load_v(f, vbase + (size_t)t * V_TILE, voff);
            f32x16 s = qk(f, qa1, qb1);
            const float mn = fmaxf(m, max16(s)), al = __builtin_amdgcn_exp2f(m - mn);
            m = mn;
#pragma unroll
            for (int e = 0; e < 16; ++e) { s[e] = __builtin_amdgcn_exp2f(s[e] - mn); oo[e] *= al; }
            oo = MFMA32(f.v0, pack8(s, 0), oo); oo = MFMA32(f.v1, pack8(s, 1), oo);
        }
#pragma unroll
        for (int g = 0; g < 3; ++g) own[g] = f32x4{oo[4 * g], oo[4 * g + 1], oo[4 * g + 2], oo[4 * g + 3]};
    }
    const float lx = xhalf(own[2][0]);
    const float rl = 1.f / (hh ? own[2][0] : lx);
    const int token = qt_idx * 32 + r;
    gp ob = (gp)ws + WS_OF + (size_t)(token >> 4) * (20 * 1024) + (token & 15) * 16;
#pragma unroll
    for (int g = 0; g < 3; ++g) {
        const int u = head * 5 + 2 * g + hh;
        const u32x2 v = {cvtpk(own[g][0] * rl, own[g][1] * rl), cvtpk(own[g][2] * rl, own[g][3] * rl)};
        if (g < 2 || hh == 0) *(GAS u32x2*)(ob + (u >> 3) * 1024 + (u & 3) * 256 + ((u >> 2) & 1) * 8) = v;
    }
}

struct HwBar { DI void operator()() const { asm volatile("s_waitcnt lgkmcnt(0)\n\ts_barrier" ::: "memory"); } };

__global__ __launch_bounds__(512, 2) void k2a_attn(char* __restrict__ ws) {
    extern __shared__ __attribute__((aligned(16))) char smem[];
    const int bid = blockIdx.x, idx = bid >> 3;
    attn_body(ws, (ldsp)smem, (bid & 7) * 4 + (idx >> 3), idx & 7, __builtin_amdgcn_readfirstlane(threadIdx.x >> 6), HwBar());
}

constexpr int GU_SLOT = 24576;
template <class BarT>
DI void gateup_body(const char* wg, const char* wu, char* ws, ldsp lds, int rb, int cb, int gw, BarT bar) {
    const int lane = threadIdx.x & 63, hh = lane >> 5;
    const unsigned lds0 = (unsigned)(uintptr_t)lds;
    const int rt = rb * 4 + gw;
    const unsigned voffB = (lane >> 2) * (FF * 2) + (lane & 3) * 16, voffA = lane * 16;
    const char* bg = wg + (size_t)(gw * 16) * (FF * 2) + cb * 192;
    const char* bu = wu + (size_t)(gw * 16) * (FF * 2) + cb * 192;
    const char* ba = ws + WS_XN + (size_t)rt * (40 * 1024);
    auto stageB = [&](int s) {
        const unsigned dst = lds0 + (s % 3) * GU_SLOT + gw * 1024;
#pragma unroll
        for (int c = 0; c < 6; ++c) glds16(voffB, (c < 3 ? bg : bu) + (size_t)(s * 64) * (FF * 2) + (c % 3) * 64, dst + c * 4096);
    };
    bf16x8 a[2][4];
    auto loadA = [&](int s) {
#pragma unroll
        for (int k = 0; k < 4; ++k) gld16(a[s & 1][k], voffA, ba + (s * 4 + k) * 1024);
    };
    stageB(0); loadA(0); stageB(1);
    const int blk = (lane >> 4) & 1, q = (lane & 15) >> 2, p = lane & 3;
    ldsp pb = lds + (8 * hh + q) * 64 + blk * 32 + p * 8;
    f32x16 acc[6] = {};
#pragma unroll
    for (int s = 0; s < 10; ++s) {
        if (s < 9) WAITV(6); else WAITV(0);
        asm volatile("" : "+v"(a[s & 1][0]), "+v"(a[s & 1][1]), "+v"(a[s & 1][2]), "+v"(a[s & 1][3]));
        bar();
        if (s + 1 < 10) loadA(s + 1);
        if (s + 2 < 10) stageB(s + 2);
        const int so = (s % 3) * GU_SLOT;
#pragma unroll
        for (int k = 0; k < 4; ++k)
#pragma unroll
            for (int c = 0; c < 6; ++c) {
                const bf16x8 b = cat(ldstr(pb + so + c * 4096 + k * 1024), ldstr(pb + so + c * 4096 + k * 1024 + 256));
                acc[c] = MFMA32(b, a[s & 1][k], acc[c]);
            }
    }
    gp hb = (gp)ws + WS_HF + (size_t)(rt * 96 + cb * 6) * 1024 + lane * 16;
#pragma unroll
    for (int c = 0; c < 3; ++c) {
        f32x16 h;
#pragma unroll
        for (int e = 0; e < 16; ++e) h[e] = fmaxf(acc[c][e], 0.f) * acc[c + 3][e];
        *(GAS bf16x8*)(hb + (2 * c) * 1024) = pack8(h, 0);
        *(GAS bf16x8*)(hb + (2 * c + 1) * 1024) = pack8(h, 1);
    }
}
__global__ __launch_bounds__(256, 3) void k2b_gateup(const char* __restrict__ wg, const char* __restrict__ wu, char* __restrict__ ws) {
    extern __shared__ __attribute__((aligned(16))) char smem[];
    const int bid = blockIdx.x, xcd = bid & 7, idx = bid >> 3;
    gateup_body(wg, wu, ws, (ldsp)smem, (xcd >> 1) * 4 + (idx & 3), (xcd & 1) * 8 + (idx >> 2), __builtin_amdgcn_readfirstlane(threadIdx.x >> 6), HwBar());
}

constexpr int DN_SLOT = 5120, DN_WAVE = 4 * DN_SLOT;
template <class BarT>
DI void down_body(const char* wd, char* ws, ldsp lds, int rt, int nb, int gw, BarT bar) {
    const int lane = threadIdx.x & 63, r = lane & 31, hh = lane >> 5;
    ldsp my = lds + gw * DN_WAVE;
    const unsigned my0 = (unsigned)(uintptr_t)lds + gw * DN_WAVE;
    const unsigned voffB = (lane >> 2) * (D * 2) + (lane & 3) * 16, voffA = lane * 16;
    const char* bb = wd + (size_t)(gw * 384) * (D * 2) + nb * 320;
    const char* ba = ws + WS_HF + (size_t)(rt * 96 + gw * 24) * 1024;
    bf16x8 a[4];
    auto issue = [&](int s) {
#pragma unroll
        for (int c = 0; c < 5; ++c) glds16(voffB, bb + (size_t)(s * 16) * (D * 2) + c * 64, my0 + (s & 3) * DN_SLOT + c * 1024);
        gld16(a[s & 3], voffA, ba + s * 1024);
    };
    issue(0); issue(1); issue(2);
    const int blk = (lane >> 4) & 1, q = (lane & 15) >> 2, p = lane & 3;
    ldsp pb = my + (4 * hh + q) * 64 + blk * 32 + p * 8;
    f32x16 acc[5] = {};
#pragma unroll
    for (int s = 0; s < 24; ++s) {
        if (s < 22) WAITV(12); else if (s == 22) WAITV(6); else WAITV(0);
        asm volatile("s_waitcnt lgkmcnt(0)" : "+v"(a[s & 3]) :: "memory");
        if (s + 3 < 24) issue(s + 3);
#pragma unroll
        for (int c = 0; c < 5; ++c) {
            const bf16x8 b = cat(ldstr(pb + (s & 3) * DN_SLOT + c * 1024), ldstr(pb + (s & 3) * DN_SLOT + c * 1024 + 512));
            acc[c] = MFMA32(b, a[s & 3], acc[c]);
        }
    }
    asm volatile("s_waitcnt lgkmcnt(0)" ::: "memory");
#pragma unroll
    for (int g = 0; g < 4; ++g)
#pragma unroll
        for (int c = 0; c < 5; ++c) *(LAS f32x4*)(my + g * 5120 + c * 1024 + lane * 16) = f32x4{acc[c][4 * g], acc[c][4 * g + 1], acc[c][4 * g + 2], acc[c][4 * g + 3]};
    bar();
    f32x4 own[5];
#pragma unroll
    for (int c = 0; c < 5; ++c) {
        ldsp src = lds + gw * 5120 + c * 1024 + lane * 16;
        own[c] = (*(const LAS f32x4*)(src) + *(const LAS f32x4*)(src + DN_WAVE)) + (*(const LAS f32x4*)(src + 2 * DN_WAVE) + *(const LAS f32x4*)(src + 3 * DN_WAVE));
    }
    gp yb = (gp)ws + WS_Y + (size_t)(rt * 32 + r) * (D * 2) + (nb * 160 + gw * 8 + hh * 4) * 2;
#pragma unroll
    for (int c = 0; c < 5; ++c) *(GAS u32x2*)(yb + c * 64) = u32x2{cvtpk(own[c][0], own[c][1]), cvtpk(own[c][2], own[c][3])};
}
__global__ __launch_bounds__(256, 3) void k2c_down(const char* __restrict__ wd, char* __restrict__ ws) {
    extern __shared__ __attribute__((aligned(16))) char smem[];
    const int bid = blockIdx.x, xcd = bid & 7, idx = bid >> 3;
    down_body(wd, ws, (ldsp)smem, xcd * 8 + (idx >> 2), idx & 3, __builtin_amdgcn_readfirstlane(threadIdx.x >> 6), HwBar());
}

constexpr int K3_WAVE = 25600, K3_LDS = 4 * K3_WAVE;
__global__ __launch_bounds__(256, 2) void k3_out(const char* __restrict__ x, const char* __restrict__ wo, const char* __restrict__ ws, char* __restrict__ out) {
    extern __shared__ __attribute__((aligned(16))) char smem[];
    ldsp lds = (ldsp)smem;
    const int bid = blockIdx.x, xcd = bid & 7, idx = bid >> 3, mb = xcd * 4 + (idx >> 3), nb = idx & 7;
    const int lane = threadIdx.x & 63, r = lane & 15, g4 = lane >> 4, w = __builtin_amdgcn_readfirstlane(threadIdx.x >> 6);
    ldsp my = lds + w * K3_WAVE;
    const unsigned my0 = (unsigned)(uintptr_t)lds + w * K3_WAVE;
    unsigned voffB[5];
#pragma unroll
    for (int i = 0; i < 5; ++i) { const int ch = i * 64 + lane, row = ch / 10; voffB[i] = row * (D * 2) + (ch - row * 10) * 16; }
    const char* bb = wo + (size_t)(w * 160) * (D * 2) + nb * 160;
    const char* ba = ws + WS_OF + (size_t)(mb * 4) * (20 * 1024) + (w * 5) * 1024;
    bf16x8 a[5][4];
#pragma unroll
    for (int ks = 0; ks < 5; ++ks) {
#pragma unroll
        for (int i = 0; i < 5; ++i) glds16(voffB[i], bb + (size_t)(ks * 32) * (D * 2), my0 + ks * 5120 + i * 1024);
#pragma unroll
        for (int mt = 0; mt < 4; ++mt) gld16(a[ks][mt], lane * 16, ba + ((mt + w) & 3) * (20 * 1024) + ks * 1024);
    }
    const size_t eoff = (size_t)(mb * 64 + w * 16 + r) * (D * 2) + (nb * 80 + g4 * 4) * 2;
    u32x2 xr[5], yr[5];
#pragma unroll
    for (int c = 0; c < 5; ++c) { xr[c] = *(const GAS u32x2*)((gcp)x + eoff + c * 32); yr[c] = *(const GAS u32x2*)((gcp)ws + WS_Y + eoff + c * 32); }
    const int q = (lane & 15) >> 2, p = lane & 3;
    ldsp pb = my + (4 * g4 + q) * 160 + p * 8;
    f32x4 acc[4][5] = {};
#pragma unroll
    for (int ks = 0; ks < 5; ++ks) {
        if (ks == 0) WAITV(36); else if (ks == 1) WAITV(27); else if (ks == 2) WAITV(18); else if (ks == 3) WAITV(9); else WAITV(0);
        asm volatile("" : "+v"(a[ks][0]), "+v"(a[ks][1]), "+v"(a[ks][2]), "+v"(a[ks][3]));
#pragma unroll
        for (int c = 0; c < 5; ++c) {
            const bf16x8 b = cat(ldstr(pb + ks * 5120 + c * 32), ldstr(pb + ks * 5120 + 16 * 160 + c * 32));
#pragma unroll
            for (int mt = 0; mt < 4; ++mt) acc[mt][c] = MFMA16(b, a[ks][mt], acc[mt][c]);
        }
    }
    asm volatile("s_waitcnt lgkmcnt(0)" ::: "memory");
#pragma unroll
    for (int mt = 1; mt < 4; ++mt)
#pragma unroll
        for (int c = 0; c < 5; ++c) *(LAS f32x4*)(my + (mt - 1) * 5120 + c * 1024 + lane * 16) = acc[mt][c];
    f32x4 own[5];
#pragma unroll
    for (int c = 0; c < 5; ++c) own[c] = acc[0][c];
    __syncthreads();
#pragma unroll
    for (int mt = 1; mt < 4; ++mt)
#pragma unroll
        for (int c = 0; c < 5; ++c) own[c] += *(const LAS f32x4*)(lds + ((w - mt) & 3) * K3_WAVE + (mt - 1) * 5120 + c * 1024 + lane * 16);
#pragma unroll
    for (int c = 0; c < 5; ++c) {
        const float v0 = rbf(bf_lo(xr[c][0]) + rbf(own[c][0])) + bf_lo(yr[c][0]), v1 = rbf(bf_hi(xr[c][0]) + rbf(own[c][1])) + bf_hi(yr[c][0]);
        const float v2 = rbf(bf_lo(xr[c][1]) + rbf(own[c][2])) + bf_lo(yr[c][1]), v3 = rbf(bf_hi(xr[c][1]) + rbf(own[c][3])) + bf_hi(yr[c][1]);
        *(GAS u32x2*)((gp)out + eoff + c * 32) = u32x2{cvtpk(v0, v1), cvtpk(v2, v3)};
    }
}

extern "C" void kernel_launch(void* const* d_in, const int* in_sizes, int n_in, void* d_out, int out_size, void* d_ws, size_t ws_size,
                              hipStream_t stream) {
    const char* x = (const char*)d_in[0];
    const char* wqkv = (const char*)d_in[1];
    const char* wo = (const char*)d_in[2];
    const char* wg = (const char*)d_in[4];
    const char* wu = (const char*)d_in[5];
    const char* wd = (const char*)d_in[6];
    char* ws = (char*)d_ws;
    (void)hipFuncSetAttribute((const void*)k1_qkv, hipFuncAttributeMaxDynamicSharedMemorySize, K1_LDS);
    (void)hipFuncSetAttribute((const void*)k2a_attn, hipFuncAttributeMaxDynamicSharedMemorySize, ATT_LDS);
    (void)hipFuncSetAttribute((const void*)k2b_gateup, hipFuncAttributeMaxDynamicSharedMemorySize, 3 * GU_SLOT);
    (void)hipFuncSetAttribute((const void*)k2c_down, hipFuncAttributeMaxDynamicSharedMemorySize, 4 * DN_WAVE);
    (void)hipFuncSetAttribute((const void*)k3_out, hipFuncAttributeMaxDynamicSharedMemorySize, K3_LDS);
    k1_qkv<<<256, 256, K1_LDS, stream>>>(x, wqkv, ws);
    k2a_attn<<<256, 512, ATT_LDS, stream>>>(ws);
    k2b_gateup<<<256, 256, 3 * GU_SLOT, stream>>>(wg, wu, ws);
    k2c_down<<<256, 256, 4 * DN_WAVE, stream>>>(wd, ws);
    k3_out<<<256, 256, K3_LDS, stream>>>(x, wo, ws, (char*)d_out);
}
```

```cpp
#include <hip/hip_runtime.h>
#include <hip/hip_bf16.h>
#include <stdint.h>

typedef short bf16x8 __attribute__((ext_vector_type(8)));
typedef short bf16x4 __attribute__((ext_vector_type(4)));
typedef float f32x32 __attribute__((ext_vector_type(32)));
typedef float f32x16 __attribute__((ext_vector_type(16)));
typedef float f32x4 __attribute__((ext_vector_type(4)));
typedef float f32x2 __attribute__((ext_vector_type(2)));
typedef unsigned u32x32 __attribute__((ext_vector_type(32)));
typedef unsigned u32x16 __attribute__((ext_vector_type(16)));
typedef unsigned u32x4 __attribute__((ext_vector_type(4)));
typedef unsigned u32x2 __attribute__((ext_vector_type(2)));
typedef __bf16 bf16v2 __attribute__((ext_vector_type(2)));
typedef unsigned long long u64;
#define LAS __attribute__((address_space(3)))
#define GAS __attribute__((address_space(1)))
#define DI __device__ __forceinline__
typedef LAS char* ldsp;
typedef const GAS char* gcp;
typedef GAS char* gp;

constexpr int S = 2048, D = 640, DH = 20, FF = 1536;
constexpr size_t TILE = 40960;
constexpr size_t DUO = (size_t)S * 16;
constexpr size_t WS_QF = 0;
constexpr size_t WS_KF = WS_QF + (size_t)S * D * 2;
constexpr size_t WS_VF = WS_KF + (size_t)S * D * 2;
constexpr size_t WS_HF = WS_VF + (size_t)S * D * 2;
constexpr size_t WS_OF = WS_HF + (size_t)S * FF * 2;
constexpr size_t WS_XN = WS_OF + (size_t)S * D * 2;
constexpr size_t WS_WF = WS_XN + (size_t)S * D * 2;
constexpr int FS = 128;
constexpr size_t WS_PFLAG = WS_WF + (size_t)136 * 20 * 1024;
constexpr size_t WS_HFLAG = WS_PFLAG + 256 * FS;
constexpr size_t WS_OFLAG = WS_HFLAG + 256 * FS;
constexpr size_t WS_RFLAG = WS_OFLAG + 256 * FS;
constexpr size_t WS_EPOCH = WS_RFLAG + 256 * FS;
constexpr size_t WS_STAT = WS_EPOCH + 128;
constexpr size_t WS_END = WS_STAT + S * 64;
constexpr float QSCALE = 0.22360679774997896f * 1.4426950408889634f;
constexpr int P_SLOT = 32768, P_NS = 4;
constexpr int L_A = 0;
constexpr int L_G = 77824;
constexpr int L_Q = 147456;
constexpr int L_SYNC = 159744, L_TICK = L_SYNC + 32, L_RS = L_SYNC + 64, L_CS = L_RS + 512, L_TOTAL = L_CS + 2048;

#define MFMA32(a, b, c) __builtin_amdgcn_mfma_f32_32x32x16_bf16((a), (b), (c), 0, 0, 0)
#define MFMA32K8(a, b, c) __builtin_amdgcn_mfma_f32_32x32x8bf16_1k((a), (b), (c), 0, 0, 0)
#define MFMA16(a, b, c) __builtin_amdgcn_mfma_f32_16x16x32_bf16((a), (b), (c), 0, 0, 0)
#define SBAR() __builtin_amdgcn_sched_barrier(0)
#define WAITV(n) asm volatile("s_waitcnt vmcnt(" #n ")" ::: "memory")
#define PIN5(a) asm volatile("" : "+v"(a[0]), "+v"(a[1]), "+v"(a[2]), "+v"(a[3]), "+v"(a[4]))
#define PRIO(n) __builtin_amdgcn_s_setprio(n)
#define RLX_AGENT __ATOMIC_RELAXED, __HIP_MEMORY_SCOPE_AGENT

template <int I> struct IC { static constexpr int v = I; };
template <int B, int E, class F> DI void sfor(F&& f) { if constexpr (B < E) { f(IC<B>{}); sfor<B + 1, E>(f); } }

DI unsigned cvtpk(float lo, float hi) { f32x2 f = {lo, hi}; return __builtin_bit_cast(unsigned, __builtin_convertvector(f, bf16v2)); }
DI float bf_lo(unsigned u) { return __uint_as_float(u << 16); }
DI float bf_hi(unsigned u) { return __uint_as_float(u & 0xffff0000u); }
DI float rbf(float x) { return bf_lo(cvtpk(x, x)); }
DI f32x2 halves(float v) {
    auto rr = __builtin_amdgcn_permlane32_swap(__float_as_uint(v), __float_as_uint(v), false, false);
    return f32x2{__uint_as_float(rr[0]), __uint_as_float(rr[1])};
}
DI int lane_id() { int l; asm volatile("v_mbcnt_lo_u32_b32 %0, -1, 0\n\tv_mbcnt_hi_u32_b32 %0, -1, %0" : "=v"(l)); return l; }
DI u32x4 make_srd(const void* p, unsigned bytes) { const u64 a = (u64)p; return u32x4{(unsigned)a, (unsigned)(a >> 32) & 0xffffu, bytes, 0x00020000u}; }
template <int SI, int LI, bool DEV = false> DI void dma16(unsigned voff, u32x4 srd, unsigned soff, unsigned ldsb) {
    unsigned t;
    if constexpr (DEV) asm volatile("s_add_u32 m0, %3, %4\n\ts_add_u32 %0, %5, %6\n\tbuffer_load_dwordx4 %1, %2, %0 offen sc1 lds"
                                    : "=&s"(t) : "v"(voff), "s"(srd), "s"(ldsb), "i"(LI), "s"(soff), "i"(SI) : "scc", "memory");
    else asm volatile("s_add_u32 m0, %3, %4\n\ts_add_u32 %0, %5, %6\n\tbuffer_load_dwordx4 %1, %2, %0 offen lds"
                      : "=&s"(t) : "v"(voff), "s"(srd), "s"(ldsb), "i"(LI), "s"(soff), "i"(SI) : "scc", "memory");
}
DI void bld16_dev(bf16x8& dst, unsigned voff, u32x4 srd, unsigned so0, unsigned so1) {
    unsigned t;
    asm volatile("s_add_u32 %1, %4, %5\n\tbuffer_load_dwordx4 %0, %2, %3, %1 offen sc1" : "=v"(dst), "=&s"(t) : "v"(voff), "s"(srd), "s"(so0), "s"(so1) : "scc", "memory");
}
DI void ld16_dev(bf16x8& dst, unsigned voff, const void* sbase) { asm volatile("global_load_dwordx4 %0, %1, %2 sc1" : "=v"(dst) : "v"(voff), "s"(sbase) : "memory"); }
DI void ld8_dev(u32x2& dst, unsigned voff, const void* sbase) { asm volatile("global_load_dwordx2 %0, %1, %2 sc1" : "=v"(dst) : "v"(voff), "s"(sbase) : "memory"); }
DI void ld4_dev(unsigned& dst, unsigned voff, const void* sbase) { asm volatile("global_load_dword %0, %1, %2 sc1" : "=v"(dst) : "v"(voff), "s"(sbase) : "memory"); }
DI void st16_dev(unsigned voff, void* sbase, bf16x8 v) { asm volatile("global_store_dwordx4 %0, %1, %2 sc1\n\ts_nop 1" ::"v"(voff), "v"(v), "s"(sbase) : "memory"); }
DI void st8_dev(unsigned voff, void* sbase, u32x2 v) { asm volatile("global_store_dwordx2 %0, %1, %2 sc1" ::"v"(voff), "v"(v), "s"(sbase) : "memory"); }
DI bf16x8 lds128(ldsp p) { return *(const LAS bf16x8*)p; }
DI bf16x4 ldstr(ldsp p) { return __builtin_amdgcn_ds_read_tr16_b64_v4i16((LAS bf16x4*)p); }
DI bf16x8 cat(bf16x4 a, bf16x4 b) { return __builtin_shufflevector(a, b, 0, 1, 2, 3, 4, 5, 6, 7); }
DI bf16x8 pack8(const f32x16& x, int s) {
    u32x4 p = {cvtpk(x[8 * s], x[8 * s + 1]), cvtpk(x[8 * s + 2], x[8 * s + 3]), cvtpk(x[8 * s + 4], x[8 * s + 5]), cvtpk(x[8 * s + 6], x[8 * s + 7])};
    return __builtin_bit_cast(bf16x8, p);
}
DI bf16x8 colsel(int lane) {
    const unsigned w = ((lane & 3) == 0 && ((lane >> 2) & 1) == ((lane >> 4) & 1)) ? 0x3F803F80u : 0u;
    return __builtin_bit_cast(bf16x8, u32x4{w, w, w, w});
}

constexpr unsigned SPIN_CAP = 1u << 20;
#define HWBAR() asm volatile("s_waitcnt lgkmcnt(0)\n\ts_barrier" ::: "memory")
DI void lds_signal(ldsp word) {
    asm volatile("s_waitcnt lgkmcnt(0)\n\ts_mov_b64 exec, 1\n\tds_add_u32 %0, %1\n\ts_mov_b64 exec, -1" ::"v"((unsigned)(uintptr_t)word), "v"(1u) : "memory");
}
template <bool NAP = true> DI void lds_wait(ldsp word, unsigned target) {
#pragma unroll 1
    for (unsigned spins = 0; spins < SPIN_CAP; ++spins) {
        unsigned v;
        asm volatile("ds_read_b32 %0, %1\n\ts_waitcnt lgkmcnt(0)" : "=v"(v) : "v"((unsigned)(uintptr_t)word) : "memory");
        if ((unsigned)__builtin_amdgcn_readfirstlane(v) >= target) break;
        if (NAP) __builtin_amdgcn_s_sleep(1);
    }
}
DI void publish(gp flag, ldsp cnt, u64 tag) {
    unsigned old;
    asm volatile("s_waitcnt vmcnt(0)\n\ts_mov_b64 exec, 1\n\tds_add_rtn_u32 %0, %1, %2\n\ts_waitcnt lgkmcnt(0)\n\ts_mov_b64 exec, -1"
                 : "=&v"(old) : "v"((unsigned)(uintptr_t)cnt), "v"(1u) : "memory");
    if (__builtin_amdgcn_readfirstlane(old) == 3) { const int l = lane_id(); if (l < FS / 8) __hip_atomic_store((GAS u64*)flag + l, tag, RLX_AGENT); }
}
DI void await(gcp flag, u64 tag) {
#pragma unroll 1
    for (unsigned spins = 0; spins < SPIN_CAP; ++spins) {
        if (__all(__hip_atomic_load((const GAS u64*)flag, RLX_AGENT) == tag)) break;
        __builtin_amdgcn_s_sleep(2);
    }
    asm volatile("" ::: "memory");
}
DI void await2(const char* flag, unsigned voff, u64 tag, u32x2& keep0, u32x2& keep1) {
    unsigned cap;
    asm volatile("s_mov_b32 %2, 0x80000\n\tglobal_load_dwordx2 %0, %5, %3 sc1\n\ts_sleep 4\n"
                 "1:\n\tglobal_load_dwordx2 %1, %5, %3 sc1\n\ts_waitcnt vmcnt(1)\n\tv_cmp_ne_u64 vcc, %4, %0\n\ts_cbranch_vccz 2f\n\t"
                 "global_load_dwordx2 %0, %5, %3 sc1\n\ts_waitcnt vmcnt(1)\n\tv_cmp_ne_u64 vcc, %4, %1\n\ts_cbranch_vccz 2f\n\ts_sub_u32 %2, %2, 1\n\ts_cbranch_scc0 1b\n2:"
                 : "=&v"(keep0), "=&v"(keep1), "=&s"(cap) : "s"(flag), "s"(tag), "v"(voff) : "vcc", "scc", "memory");
}
template <int PR> DI void flag_check(u32x2 v, const char* flag, unsigned voff, u64 tag) {
    unsigned cap;
    asm volatile("s_setprio 2\n\ts_mov_b32 %1, 0x100000\n"
                 "1:\n\tv_cmp_ne_u64 vcc, %3, %0\n\ts_cbranch_vccz 2f\n\ts_sleep 2\n\tglobal_load_dwordx2 %0, %4, %2 sc1\n\ts_sub_u32 %1, %1, 1\n\ts_waitcnt vmcnt(0)\n\ts_cbranch_scc0 1b\n2:\n\ts_setprio %5"
                 : "+v"(v), "=&s"(cap) : "s"(flag), "s"(tag), "v"(voff), "i"(PR) : "vcc", "scc", "memory");
}
DI void tick(ldsp tp, unsigned one, u64 mask) {
    asm volatile("s_mov_b64 exec, %2\n\tds_add_u32 %0, %1\n\ts_mov_b64 exec, -1" ::"v"((unsigned)(uintptr_t)tp), "v"(one), "s"(mask) : "memory");
}
DI unsigned tick_peek(ldsp tp) { return *(volatile LAS unsigned*)tp; }
template <int PR> DI void tick_wait(unsigned t, ldsp tp, unsigned need) {
    unsigned cap;
    asm volatile("s_setprio 2\n\ts_mov_b32 %1, 0x100000\n"
                 "1:\n\tv_cmp_gt_u32 vcc, %3, %0\n\ts_cbranch_vccz 2f\n\ts_sleep 1\n\tds_read_b32 %0, %2\n\ts_sub_u32 %1, %1, 1\n\ts_waitcnt lgkmcnt(0)\n\ts_cbranch_scc0 1b\n2:\n\ts_setprio %4"
                 : "+v"(t), "=&s"(cap) : "v"((unsigned)(uintptr_t)tp), "s"(need), "i"(PR) : "vcc", "scc", "memory");
}
DI unsigned load_stat(const char* ws, int token, u64 tag, u32x2 early) {
    if (__all(early[1] == (unsigned)tag)) return early[0];
    u64 g = 0;
#pragma unroll 1
    for (unsigned spins = 0; spins < SPIN_CAP; ++spins) {
        g = __hip_atomic_load((const GAS u64*)((gcp)ws + WS_STAT + token * 64), RLX_AGENT);
        if (__all((unsigned)(g >> 32) == (unsigned)tag)) break;
        __builtin_amdgcn_s_sleep(2);
    }
    return (unsigned)g;
}
DI bf16x4 negmean_op(unsigned st) { const unsigned nm = (st & 0xffffu) ^ 0x8000u; return __builtin_bit_cast(bf16x4, u32x2{nm | (nm << 16), nm}); }
DI bf16x4 colsum_op(float c) {
    const float hi = bf_hi(__float_as_uint(c)), r1 = c - hi, mid = bf_hi(__float_as_uint(r1));
    return __builtin_bit_cast(bf16x4, u32x2{(__float_as_uint(hi) >> 16) | __float_as_uint(mid), cvtpk(r1 - mid, 0.f)});
}
template <int CTRL> DI float dpp_add(float v) { return v + __uint_as_float(__builtin_amdgcn_update_dpp(0u, __float_as_uint(v), CTRL, 0xf, 0xf, true)); }
DI float sum32(float v) {
    v = dpp_add<0xB1>(v); v = dpp_add<0x4E>(v); v = dpp_add<0x141>(v); v = dpp_add<0x140>(v);
    const auto t = __builtin_amdgcn_permlane16_swap(__float_as_uint(v), __float_as_uint(v), false, false);
    return __uint_as_float(t[0]) + __uint_as_float(t[1]);
}
DI u64 awave_start(const char* x, char* ws, ldsp lds, int srow, int wid, u32x32& pv, f32x32& accA, f32x32& accB) {
    const int lane = lane_id(), r = lane & 31, hh = lane >> 5, wm = wid >> 1, wn = wid & 1;
    const unsigned vo = hh * (D * 2) + r * 16, vo2 = hh * (D * 2) + 1024 + (r & 15) * 16;
    u32x2 ep; bf16x8 xs[3];
    ld16_dev(xs[0], vo, x + (size_t)srow * (D * 2)); ld16_dev(xs[1], vo, x + (size_t)srow * (D * 2) + 512); ld16_dev(xs[2], vo2, x + (size_t)srow * (D * 2));
    ld8_dev(ep, 0u, ws + WS_EPOCH);
    const int blk = (lane >> 4) & 1, q = (lane & 15) >> 2, p = lane & 3;
    const unsigned lds0 = (unsigned)(uintptr_t)lds, pa0 = lds0 + wm * 8192 + r * 128, sz = (r >> 1) & 7, pb0 = lds0 + (8 * hh + q) * 256 + 8 * (p & 1);
    pv = u32x32{pa0 + ((hh ^ sz) << 4), pa0 + (((2 + hh) ^ sz) << 4), pa0 + (((4 + hh) ^ sz) << 4), pa0 + (((6 + hh) ^ sz) << 4),
                pb0 + (((8 * wn + 2 * blk + (p >> 1)) ^ (q << 2)) << 4), pb0 + (((8 * wn + 4 + 2 * blk + (p >> 1)) ^ (q << 2)) << 4),
                (unsigned)(hh * 64 + (lane & 7) * 8), (unsigned)lane * 64, 0u, 0u, 0u, 0u, vo, vo2};
    accA = f32x32{}; accB = f32x32{};
    asm volatile("s_waitcnt vmcnt(0)" : "+{v[0:31]}"(accA), "+{v[32:63]}"(accB), "+{v[128:159]}"(pv), "+v"(ep), "+v"(xs[0]), "+v"(xs[1]), "+v"(xs[2]) :: "memory");
    const u64 tag = (((u64)(unsigned)__builtin_amdgcn_readfirstlane(ep[1]) << 32) | (unsigned)__builtin_amdgcn_readfirstlane(ep[0])) + 1ull;
    float v[24], s0 = 0.f, s1 = 0.f, s2 = 0.f;
#pragma unroll
    for (int i = 0; i < 12; ++i) { const unsigned u = __builtin_bit_cast(u32x4, xs[i >> 2])[i & 3]; v[2 * i] = bf_lo(u); v[2 * i + 1] = bf_hi(u); }
#pragma unroll
    for (int i = 0; i < 8; ++i) { s0 += v[i]; s1 += v[8 + i]; s2 += v[16 + i]; }
    const float mean = sum32(fmaf(s2, 0.5f, s0 + s1)) * (1.f / D);
    s0 = 0.f; s1 = 0.f; s2 = 0.f;
#pragma unroll
    for (int i = 0; i < 8; ++i) { s0 = fmaf(v[i] - mean, v[i] - mean, s0); s1 = fmaf(v[8 + i] - mean, v[8 + i] - mean, s1); s2 = fmaf(v[16 + i] - mean, v[16 + i] - mean, s2); }
    const unsigned md = cvtpk(mean, sqrtf(rbf(rbf(sum32(fmaf(s2, 0.5f, s0 + s1)) * (1.f / D)) + 1e-5f)));
    const float mb = bf_lo(md), rs = 1.f / bf_hi(md);
#pragma unroll
    for (int i = 0; i < 12; ++i) { const unsigned c = cvtpk(v[2 * i] - mb, v[2 * i + 1] - mb); pv[16 + i] = cvtpk(bf_lo(c) * rs, bf_hi(c) * rs); }
    pv[8] = md; pv[9] = (unsigned)tag;
    return tag;
}
DI void retile_fetch(const char* wd, const char* wo, unsigned ldb, int u, int lane) {
    const unsigned voff = (lane >> 4) * (D * 2) + (((lane & 15) ^ ((lane >> 4) << 2)) << 4);
    const int ks = u / 5, cg = u - ks * 5, j = (ks - 96) % 10, qd = (ks - 96) / 10;
    const bool isd = ks < 96;
    const int row0 = isd ? ks * 16 : qd * 160 + (j < 8 ? j * 20 : (j - 8) * 80 + 16), rstep = (!isd && j >= 8) ? 20 : 4;
    const u32x4 srd = make_srd(isd ? wd : wo, isd ? FF * D * 2 : D * D * 2);
    const unsigned so = row0 * (D * 2) + cg * 256, ss = rstep * (D * 2);
    dma16<0, 0>(voff, srd, so, ldb); dma16<0, 1024>(voff, srd, so + ss, ldb); dma16<0, 2048>(voff, srd, so + 2 * ss, ldb); dma16<0, 3072>(voff, srd, so + 3 * ss, ldb);
}
DI void retile_store(char* ws, ldsp img, int u, int lane) {
    const int hh = lane >> 5, blk = (lane >> 4) & 1, q = (lane & 15) >> 2, p = lane & 3;
#pragma unroll
    for (int c = 0; c < 4; ++c) {
        ldsp a = img + (4 * hh + q) * 256 + 8 * (p & 1) + (((4 * c + 2 * blk + (p >> 1)) ^ (q << 2)) << 4);
        st16_dev(lane * 16, ws + WS_WF + (size_t)(u * 4 + c) * 1024, cat(ldstr(a), ldstr(a + 2048)));
    }
}

DI void proj_body(char* ws, ldsp lds, int rb, int cb, int srow, int wid, u64 tag, u32x32 pv, f32x32 accA, f32x32 accB) {
    const int wm = wid >> 1, wn = wid & 1;
    const char* xdst = ws + WS_XN + (size_t)srow * (D * 2);
    const char* gdst = ws + WS_STAT + srow * 64;
    const char* esrc = ws + WS_STAT + (rb * 128 + wm * 64) * 64;
    asm volatile(R"ASM(
  s_waitcnt lgkmcnt(0)
  s_barrier
  s_cmp_eq_u32 s50, 15
  s_cbranch_scc1 8f
  ds_read_b128 v[64:67], v128 offset:0
  ds_read_b64_tr_b16 v[68:69], v132 offset:16384
  ds_read_b64_tr_b16 v[70:71], v132 offset:17408
  ds_read_b64_tr_b16 v[72:73], v133 offset:16384
  ds_read_b64_tr_b16 v[74:75], v133 offset:17408
  ds_read_b128 v[76:79], v128 offset:4096
  ds_read_b128 v[80:83], v129 offset:0
  ds_read_b64_tr_b16 v[84:85], v132 offset:20480
  ds_read_b64_tr_b16 v[86:87], v132 offset:21504
  ds_read_b64_tr_b16 v[88:89], v133 offset:20480
  ds_read_b64_tr_b16 v[90:91], v133 offset:21504
  ds_read_b128 v[92:95], v129 offset:4096
  s_mov_b32 s44, 0
  s_cmp_ge_u32 s50, 10
  s_cbranch_scc1 7f
1:
  s_waitcnt lgkmcnt(6)
  v_mfma_f32_32x32x16_bf16 v[0:15], v[68:71], v[64:67], v[0:15]
  ds_read_b128 v[96:99], v130 offset:0
  v_mfma_f32_32x32x16_bf16 v[16:31], v[72:75], v[64:67], v[16:31]
  ds_read_b64_tr_b16 v[100:101], v132 offset:24576
  ds_read_b64_tr_b16 v[102:103], v132 offset:25600
  v_mfma_f32_32x32x16_bf16 v[32:47], v[68:71], v[76:79], v[32:47]
  ds_read_b64_tr_b16 v[104:105], v133 offset:24576
  ds_read_b64_tr_b16 v[106:107], v133 offset:25600
  v_mfma_f32_32x32x16_bf16 v[48:63], v[72:75], v[76:79], v[48:63]
  ds_read_b128 v[108:111], v130 offset:4096
  s_cmp_lg_u32 s44, 2
  s_cbranch_scc1 2f
  s_waitcnt vmcnt(0)
  s_mov_b64 exec, s[46:47]
  global_store_dwordx2 v134, v[136:137], s[40:41] sc1
  s_mov_b64 exec, -1
2:
  s_waitcnt lgkmcnt(6)
  v_mfma_f32_32x32x16_bf16 v[0:15], v[84:87], v[80:83], v[0:15]
  ds_read_b128 v[112:115], v131 offset:0
  v_mfma_f32_32x32x16_bf16 v[16:31], v[88:91], v[80:83], v[16:31]
  ds_read_b64_tr_b16 v[116:117], v132 offset:28672
  ds_read_b64_tr_b16 v[118:119], v132 offset:29696
  v_mfma_f32_32x32x16_bf16 v[32:47], v[84:87], v[92:95], v[32:47]
  ds_read_b64_tr_b16 v[120:121], v133 offset:28672
  ds_read_b64_tr_b16 v[122:123], v133 offset:29696
  v_mfma_f32_32x32x16_bf16 v[48:63], v[88:91], v[92:95], v[48:63]
  ds_read_b128 v[124:127], v131 offset:4096
  s_cmp_lg_u32 s44, 4
  s_cbranch_scc1 3f
  global_load_dwordx2 v[138:139], v135, s[42:43] sc1
3:
  s_waitcnt lgkmcnt(6)
  v_mfma_f32_32x32x16_bf16 v[0:15], v[100:103], v[96:99], v[0:15]
  s_barrier
  ds_read_b128 v[64:67], v128 offset:32768
  v_mfma_f32_32x32x16_bf16 v[16:31], v[104:107], v[96:99], v[16:31]
  ds_read_b64_tr_b16 v[68:69], v132 offset:49152
  ds_read_b64_tr_b16 v[70:71], v132 offset:50176
  v_mfma_f32_32x32x16_bf16 v[32:47], v[100:103], v[108:111], v[32:47]
  ds_read_b64_tr_b16 v[72:73], v133 offset:49152
  ds_read_b64_tr_b16 v[74:75], v133 offset:50176
  v_mfma_f32_32x32x16_bf16 v[48:63], v[104:107], v[108:111], v[48:63]
  ds_read_b128 v[76:79], v128 offset:36864
  s_waitcnt lgkmcnt(6)
  v_mfma_f32_32x32x16_bf16 v[0:15], v[116:119], v[112:115], v[0:15]
  ds_read_b128 v[80:83], v129 offset:32768
  v_mfma_f32_32x32x16_bf16 v[16:31], v[120:123], v[112:115], v[16:31]
  ds_read_b64_tr_b16 v[84:85], v132 offset:53248
  ds_read_b64_tr_b16 v[86:87], v132 offset:54272
  v_mfma_f32_32x32x16_bf16 v[32:47], v[116:119], v[124:127], v[32:47]
  ds_read_b64_tr_b16 v[88:89], v133 offset:53248
  ds_read_b64_tr_b16 v[90:91], v133 offset:54272
  v_mfma_f32_32x32x16_bf16 v[48:63], v[120:123], v[124:127], v[48:63]
  ds_read_b128 v[92:95], v129 offset:36864
  s_cmp_lg_u32 s44, 0
  s_cbranch_scc1 5f
  global_store_dwordx4 v140, v[144:147], s[48:49] sc1
  global_store_dwordx4 v140, v[148:151], s[48:49] offset:512 sc1
  global_store_dwordx4 v141, v[152:155], s[48:49] sc1
5:
  s_waitcnt lgkmcnt(6)
  v_mfma_f32_32x32x16_bf16 v[0:15], v[68:71], v[64:67], v[0:15]
  ds_read_b128 v[96:99], v130 offset:32768
  v_mfma_f32_32x32x16_bf16 v[16:31], v[72:75], v[64:67], v[16:31]
  ds_read_b64_tr_b16 v[100:101], v132 offset:57344
  ds_read_b64_tr_b16 v[102:103], v132 offset:58368
  v_mfma_f32_32x32x16_bf16 v[32:47], v[68:71], v[76:79], v[32:47]
  ds_read_b64_tr_b16 v[104:105], v133 offset:57344
  ds_read_b64_tr_b16 v[106:107], v133 offset:58368
  v_mfma_f32_32x32x16_bf16 v[48:63], v[72:75], v[76:79], v[48:63]
  ds_read_b128 v[108:111], v130 offset:36864
  s_waitcnt lgkmcnt(6)
  v_mfma_f32_32x32x16_bf16 v[0:15], v[84:87], v[80:83], v[0:15]
  ds_read_b128 v[112:115], v131 offset:32768
  v_mfma_f32_32x32x16_bf16 v[16:31], v[88:91], v[80:83], v[16:31]
  ds_read_b64_tr_b16 v[116:117], v132 offset:61440
  ds_read_b64_tr_b16 v[118:119], v132 offset:62464
  v_mfma_f32_32x32x16_bf16 v[32:47], v[84:87], v[92:95], v[32:47]
  ds_read_b64_tr_b16 v[120:121], v133 offset:61440
  ds_read_b64_tr_b16 v[122:123], v133 offset:62464
  v_mfma_f32_32x32x16_bf16 v[48:63], v[88:91], v[92:95], v[48:63]
  ds_read_b128 v[124:127], v131 offset:36864
  s_add_u32 s44, s44, 1
  s_cmp_eq_u32 s44, 5
  v_xor_b32 v128, 0x10000, v128
  v_xor_b32 v132, 0x10000, v132
  v_xor_b32 v133, 0x10000, v133
  s_waitcnt lgkmcnt(6)
  v_mfma_f32_32x32x16_bf16 v[0:15], v[100:103], v[96:99], v[0:15]
  s_cbranch_scc1 4f
  s_barrier
4:
  ds_read_b128 v[64:67], v128 offset:0
  v_mfma_f32_32x32x16_bf16 v[16:31], v[104:107], v[96:99], v[16:31]
  ds_read_b64_tr_b16 v[68:69], v132 offset:16384
  ds_read_b64_tr_b16 v[70:71], v132 offset:17408
  v_mfma_f32_32x32x16_bf16 v[32:47], v[100:103], v[108:111], v[32:47]
  ds_read_b64_tr_b16 v[72:73], v133 offset:16384
  ds_read_b64_tr_b16 v[74:75], v133 offset:17408
  v_mfma_f32_32x32x16_bf16 v[48:63], v[104:107], v[108:111], v[48:63]
  ds_read_b128 v[76:79], v128 offset:4096
  v_xor_b32 v129, 0x10000, v129
  v_xor_b32 v130, 0x10000, v130
  v_xor_b32 v131, 0x10000, v131
  s_waitcnt lgkmcnt(6)
  v_mfma_f32_32x32x16_bf16 v[0:15], v[116:119], v[112:115], v[0:15]
  ds_read_b128 v[80:83], v129 offset:0
  v_mfma_f32_32x32x16_bf16 v[16:31], v[120:123], v[112:115], v[16:31]
  ds_read_b64_tr_b16 v[84:85], v132 offset:20480
  ds_read_b64_tr_b16 v[86:87], v132 offset:21504
  v_mfma_f32_32x32x16_bf16 v[32:47], v[116:119], v[124:127], v[32:47]
  ds_read_b64_tr_b16 v[88:89], v133 offset:20480
  ds_read_b64_tr_b16 v[90:91], v133 offset:21504
  v_mfma_f32_32x32x16_bf16 v[48:63], v[120:123], v[124:127], v[48:63]
  ds_read_b128 v[92:95], v129 offset:4096
  s_cbranch_scc0 1b
  s_branch 9f
8:
  global_store_dwordx4 v140, v[144:147], s[48:49] sc1
  global_store_dwordx4 v140, v[148:151], s[48:49] offset:512 sc1
  global_store_dwordx4 v141, v[152:155], s[48:49] sc1
  s_waitcnt vmcnt(0)
  s_mov_b64 exec, s[46:47]
  global_store_dwordx2 v134, v[136:137], s[40:41] sc1
  s_mov_b64 exec, -1
  s_branch 9f
7:
1:
  s_waitcnt lgkmcnt(6)
  v_mfma_f32_32x32x16_bf16 v[0:15], v[64:67], v[68:71], v[0:15]
  ds_read_b128 v[96:99], v130 offset:0
  v_mfma_f32_32x32x16_bf16 v[16:31], v[64:67], v[72:75], v[16:31]
  ds_read_b64_tr_b16 v[100:101], v132 offset:24576
  ds_read_b64_tr_b16 v[102:103], v132 offset:25600
  v_mfma_f32_32x32x16_bf16 v[32:47], v[76:79], v[68:71], v[32:47]
  ds_read_b64_tr_b16 v[104:105], v133 offset:24576
  ds_read_b64_tr_b16 v[106:107], v133 offset:25600
  v_mfma_f32_32x32x16_bf16 v[48:63], v[76:79], v[72:75], v[48:63]
  ds_read_b128 v[108:111], v130 offset:4096
  s_cmp_lg_u32 s44, 2
  s_cbranch_scc1 2f
  s_waitcnt vmcnt(0)
  s_mov_b64 exec, s[46:47]
  global_store_dwordx2 v134, v[136:137], s[40:41] sc1
  s_mov_b64 exec, -1
2:
  s_waitcnt lgkmcnt(6)
  v_mfma_f32_32x32x16_bf16 v[0:15], v[80:83], v[84:87], v[0:15]
  ds_read_b128 v[112:115], v131 offset:0
  v_mfma_f32_32x32x16_bf16 v[16:31], v[80:83], v[88:91], v[16:31]
  ds_read_b64_tr_b16 v[116:117], v132 offset:28672
  ds_read_b64_tr_b16 v[118:119], v132 offset:29696
  v_mfma_f32_32x32x16_bf16 v[32:47], v[92:95], v[84:87], v[32:47]
  ds_read_b64_tr_b16 v[120:121], v133 offset:28672
  ds_read_b64_tr_b16 v[122:123], v133 offset:29696
  v_mfma_f32_32x32x16_bf16 v[48:63], v[92:95], v[88:91], v[48:63]
  ds_read_b128 v[124:127], v131 offset:4096
  s_cmp_lg_u32 s44, 4
  s_cbranch_scc1 3f
  global_load_dwordx2 v[138:139], v135, s[42:43] sc1
3:
  s_waitcnt lgkmcnt(6)
  v_mfma_f32_32x32x16_bf16 v[0:15], v[96:99], v[100:103], v[0:15]
  s_barrier
  ds_read_b128 v[64:67], v128 offset:32768
  v_mfma_f32_32x32x16_bf16 v[16:31], v[96:99], v[104:107], v[16:31]
  ds_read_b64_tr_b16 v[68:69], v132 offset:49152
  ds_read_b64_tr_b16 v[70:71], v132 offset:50176
  v_mfma_f32_32x32x16_bf16 v[32:47], v[108:111], v[100:103], v[32:47]
  ds_read_b64_tr_b16 v[72:73], v133 offset:49152
  ds_read_b64_tr_b16 v[74:75], v133 offset:50176
  v_mfma_f32_32x32x16_bf16 v[48:63], v[108:111], v[104:107], v[48:63]
  ds_read_b128 v[76:79], v128 offset:36864
  s_waitcnt lgkmcnt(6)
  v_mfma_f32_32x32x16_bf16 v[0:15], v[112:115], v[116:119], v[0:15]
  ds_read_b128 v[80:83], v129 offset:32768
  v_mfma_f32_32x32x16_bf16 v[16:31], v[112:115], v[120:123], v[16:31]
  ds_read_b64_tr_b16 v[84:85], v132 offset:53248
  ds_read_b64_tr_b16 v[86:87], v132 offset:54272
  v_mfma_f32_32x32x16_bf16 v[32:47], v[124:127], v[116:119], v[32:47]
  ds_read_b64_tr_b16 v[88:89], v133 offset:53248
  ds_read_b64_tr_b16 v[90:91], v133 offset:54272
  v_mfma_f32_32x32x16_bf16 v[48:63], v[124:127], v[120:123], v[48:63]
  ds_read_b128 v[92:95], v129 offset:36864
  s_cmp_lg_u32 s44, 0
  s_cbranch_scc1 5f
  global_store_dwordx4 v140, v[144:147], s[48:49] sc1
  global_store_dwordx4 v140, v[148:151], s[48:49] offset:512 sc1
  global_store_dwordx4 v141, v[152:155], s[48:49] sc1
5:
  s_waitcnt lgkmcnt(6)
  v_mfma_f32_32x32x16_bf16 v[0:15], v[64:67], v[68:71], v[0:15]
  ds_read_b128 v[96:99], v130 offset:32768
  v_mfma_f32_32x32x16_bf16 v[16:31], v[64:67], v[72:75], v[16:31]
  ds_read_b64_tr_b16 v[100:101], v132 offset:57344
  ds_read_b64_tr_b16 v[102:103], v132 offset:58368
  v_mfma_f32_32x32x16_bf16 v[32:47], v[76:79], v[68:71], v[32:47]
  ds_read_b64_tr_b16 v[104:105], v133 offset:57344
  ds_read_b64_tr_b16 v[106:107], v133 offset:58368
  v_mfma_f32_32x32x16_bf16 v[48:63], v[76:79], v[72:75], v[48:63]
  ds_read_b128 v[108:111], v130 offset:36864
  s_waitcnt lgkmcnt(6)
  v_mfma_f32_32x32x16_bf16 v[0:15], v[80:83], v[84:87], v[0:15]
  ds_read_b128 v[112:115], v131 offset:32768
  v_mfma_f32_32x32x16_bf16 v[16:31], v[80:83], v[88:91], v[16:31]
  ds_read_b64_tr_b16 v[116:117], v132 offset:61440
  ds_read_b64_tr_b16 v[118:119], v132 offset:62464
  v_mfma_f32_32x32x16_bf16 v[32:47], v[92:95], v[84:87], v[32:47]
  ds_read_b64_tr_b16 v[120:121], v133 offset:61440
  ds_read_b64_tr_b16 v[122:123], v133 offset:62464
  v_mfma_f32_32x32x16_bf16 v[48:63], v[92:95], v[88:91], v[48:63]
  ds_read_b128 v[124:127], v131 offset:36864
  s_add_u32 s44, s44, 1
  s_cmp_eq_u32 s44, 5
  v_xor_b32 v128, 0x10000, v128
  v_xor_b32 v132, 0x10000, v132
  v_xor_b32 v133, 0x10000, v133
  s_waitcnt lgkmcnt(6)
  v_mfma_f32_32x32x16_bf16 v[0:15], v[96:99], v[100:103], v[0:15]
  s_cbranch_scc1 4f
  s_barrier
4:
  ds_read_b128 v[64:67], v128 offset:0
  v_mfma_f32_32x32x16_bf16 v[16:31], v[96:99], v[104:107], v[16:31]
  ds_read_b64_tr_b16 v[68:69], v132 offset:16384
  ds_read_b64_tr_b16 v[70:71], v132 offset:17408
  v_mfma_f32_32x32x16_bf16 v[32:47], v[108:111], v[100:103], v[32:47]
  ds_read_b64_tr_b16 v[72:73], v133 offset:16384
  ds_read_b64_tr_b16 v[74:75], v133 offset:17408
  v_mfma_f32_32x32x16_bf16 v[48:63], v[108:111], v[104:107], v[48:63]
  ds_read_b128 v[76:79], v128 offset:4096
  v_xor_b32 v129, 0x10000, v129
  v_xor_b32 v130, 0x10000, v130
  v_xor_b32 v131, 0x10000, v131
  s_waitcnt lgkmcnt(6)
  v_mfma_f32_32x32x16_bf16 v[0:15], v[112:115], v[116:119], v[0:15]
  ds_read_b128 v[80:83], v129 offset:0
  v_mfma_f32_32x32x16_bf16 v[16:31], v[112:115], v[120:123], v[16:31]
  ds_read_b64_tr_b16 v[84:85], v132 offset:20480
  ds_read_b64_tr_b16 v[86:87], v132 offset:21504
  v_mfma_f32_32x32x16_bf16 v[32:47], v[124:127], v[116:119], v[32:47]
  ds_read_b64_tr_b16 v[88:89], v133 offset:20480
  ds_read_b64_tr_b16 v[90:91], v133 offset:21504
  v_mfma_f32_32x32x16_bf16 v[48:63], v[124:127], v[120:123], v[48:63]
  ds_read_b128 v[92:95], v129 offset:4096
  s_cbranch_scc0 1b
9:
  s_setprio 3
  s_waitcnt vmcnt(0) lgkmcnt(0)
  s_nop 9
)ASM"
            : "+{v[0:31]}"(accA), "+{v[32:63]}"(accB), "+{v[128:159]}"(pv)
            : "{s[40:41]}"(gdst), "{s[42:43]}"(esrc), "{s[46:47]}"(0x000000ff000000ffull), "{s[48:49]}"(xdst), "{s50}"(cb)
            : "v64", "v65", "v66", "v67", "v68", "v69", "v70", "v71", "v72", "v73", "v74", "v75", "v76", "v77", "v78", "v79",
              "v80", "v81", "v82", "v83", "v84", "v85", "v86", "v87", "v88", "v89", "v90", "v91", "v92", "v93", "v94", "v95",
              "v96", "v97", "v98", "v99", "v100", "v101", "v102", "v103", "v104", "v105", "v106", "v107", "v108", "v109", "v110", "v111",
              "v112", "v113", "v114", "v115", "v116", "v117", "v118", "v119", "v120", "v121", "v122", "v123", "v124", "v125", "v126", "v127",
              "s44", "scc", "memory");
    if (cb < 15) {
    const int lane = lane_id(), r = lane & 31, hh = lane >> 5;
    f32x16 acc00 = __builtin_shufflevector(accA, accA, 0, 1, 2, 3, 4, 5, 6, 7, 8, 9, 10, 11, 12, 13, 14, 15);
    f32x16 acc01 = __builtin_shufflevector(accA, accA, 16, 17, 18, 19, 20, 21, 22, 23, 24, 25, 26, 27, 28, 29, 30, 31);
    f32x16 acc10 = __builtin_shufflevector(accB, accB, 0, 1, 2, 3, 4, 5, 6, 7, 8, 9, 10, 11, 12, 13, 14, 15);
    f32x16 acc11 = __builtin_shufflevector(accB, accB, 16, 17, 18, 19, 20, 21, 22, 23, 24, 25, 26, 27, 28, 29, 30, 31);
    const unsigned rdy = tick_peek(lds + L_SYNC + 16);
    bf16x4 c0 = *(const volatile LAS bf16x4*)(lds + L_CS + (wn * 128 + lane) * 8), c1 = *(const volatile LAS bf16x4*)(lds + L_CS + (wn * 128 + 64 + lane) * 8);
    const unsigned st = load_stat(ws, rb * 128 + wm * 64 + lane, tag, u32x2{pv[10], pv[11]});
    const auto sw = __builtin_amdgcn_permlane32_swap(st, st, false, false);
    if (__builtin_amdgcn_readfirstlane(rdy) < 4) {
        lds_wait(lds + L_SYNC + 16, 4);
        c0 = *(const volatile LAS bf16x4*)(lds + L_CS + (wn * 128 + lane) * 8); c1 = *(const volatile LAS bf16x4*)(lds + L_CS + (wn * 128 + 64 + lane) * 8);
    }
    {
        const bf16x4 m0 = negmean_op(sw[0]), m1 = negmean_op(sw[1]);
        const bool isv = cb >= 10;
        acc00 = MFMA32K8(isv ? m0 : c0, isv ? c0 : m0, acc00); acc01 = MFMA32K8(isv ? m0 : c1, isv ? c1 : m0, acc01);
        acc10 = MFMA32K8(isv ? m1 : c0, isv ? c0 : m1, acc10); acc11 = MFMA32K8(isv ? m1 : c1, isv ? c1 : m1, acc11);
    }
    if (cb < 10) {
        const bool isq = cb < 5; const int cbs = isq ? cb : cb - 5;
        char* dstb = ws + (isq ? WS_QF : WS_KF) + (size_t)(cbs * 16 + wn * 8) * DUO + rb * 2048 + wm * 1024;
        const float sc = isq ? QSCALE : 1.f;
        const unsigned vo = hh * (unsigned)DUO + r * 16;
#pragma unroll
        for (int i = 0; i < 2; ++i) {
            const float ca = sc * __builtin_amdgcn_rcpf(bf_hi(sw[i]));
#pragma unroll
            for (int j = 0; j < 2; ++j) {
                const f32x16& ac = i ? (j ? acc11 : acc10) : (j ? acc01 : acc00);
#pragma unroll
                for (int g = 0; g < 4; g += 2) {
                    const auto x0 = __builtin_amdgcn_permlane32_swap(cvtpk(ac[4 * g] * ca, ac[4 * g + 1] * ca), cvtpk(ac[4 * g + 4] * ca, ac[4 * g + 5] * ca), false, false);
                    const auto x1 = __builtin_amdgcn_permlane32_swap(cvtpk(ac[4 * g + 2] * ca, ac[4 * g + 3] * ca), cvtpk(ac[4 * g + 6] * ca, ac[4 * g + 7] * ca), false, false);
                    st16_dev(vo, dstb + (size_t)(j * 4 + g) * DUO + i * 512, __builtin_bit_cast(bf16x8, u32x4{x0[0], x1[0], x0[1], x1[1]}));
                }
            }
        }
    } else {
        *(LAS float*)(lds + L_RS + (wm * 64 + lane) * 4) = __builtin_amdgcn_rcpf(bf_hi(st));
        char* dstb = ws + WS_VF + (size_t)(rb * 4 + wm * 2) * TILE + ((cb - 10) * 4 + wn * 2) * 2048;
#pragma unroll
        for (int i = 0; i < 2; ++i) {
            f32x4 ca[4];
#pragma unroll
            for (int g = 0; g < 4; ++g) ca[g] = *(const LAS f32x4*)(lds + L_RS + (wm * 64 + i * 32 + 8 * g + 4 * hh) * 4);
#pragma unroll
            for (int j = 0; j < 2; ++j) {
                const f32x16& ac = i ? (j ? acc11 : acc10) : (j ? acc01 : acc00);
                f32x16 v;
#pragma unroll
                for (int e = 0; e < 16; ++e) v[e] = ac[e] * ca[e >> 2][e & 3];
                st16_dev(lane * 16, dstb + i * TILE + j * 2048, pack8(v, 0));
                st16_dev(lane * 16, dstb + i * TILE + j * 2048 + 1024, pack8(v, 1));
            }
        }
    }
    }
    lds_signal(lds + L_SYNC);
}

struct QK { bf16x8 a; unsigned b; };
DI void ld_qk(QK& f, const char* t, unsigned offa, unsigned offb) { ld16_dev(f.a, offa, t); ld4_dev(f.b, offb, t); }
#define PIN_QK(f) asm volatile("" : "+v"((f).a), "+v"((f).b))
DI bf16x8 qk_a(const QK& f) { return f.a; }
DI bf16x4 qk_b(const QK& f, unsigned extra) { return __builtin_bit_cast(bf16x4, u32x2{f.b, extra}); }
DI f32x16 score(const QK& k, const QK& q) { f32x16 z = {}; return MFMA32K8(qk_b(k, 0u), qk_b(q, 0u), MFMA32(qk_a(k), qk_a(q), z)); }
DI float max16(const f32x16& s) {
    float a = fmaxf(fmaxf(s[0], s[1]), s[2]), b = fmaxf(fmaxf(s[3], s[4]), s[5]);
    a = fmaxf(fmaxf(a, s[6]), s[7]); b = fmaxf(fmaxf(b, s[8]), s[9]); a = fmaxf(fmaxf(a, s[10]), s[11]); b = fmaxf(fmaxf(b, s[12]), s[13]);
    a = fmaxf(fmaxf(a, s[14]), s[15]);
    return fmaxf(a, b);
}
DI bf16x8 vfix(bf16x8 v, int r) {
    const u32x4 u = __builtin_bit_cast(u32x4, v); const unsigned c = r == 20 ? 0x3F803F80u : 0u; const bool k = r < 20;
    return __builtin_bit_cast(bf16x8, u32x4{k ? u[0] : c, k ? u[1] : c, k ? u[2] : c, k ? u[3] : c});
}
template <class V> DI f32x4 quad(const V& o, int i) { return f32x4{o[i], o[i + 1], o[i + 2], o[i + 3]}; }

DI unsigned v_off(int head, int r, int hh) { const int n = head * DH + (r < 20 ? r : 19); return (n >> 5) * 2048 + hh * 512 + (n & 31) * 16; }

DI void attn_body(char* ws, ldsp lds, int head, int qblk, int kq, u64 tag, int pflag) {
    int lane = lane_id(), r = lane & 31, hh = lane >> 5;
    const int odd = head & 1;
    unsigned lane8 = (hh + odd) * (unsigned)DUO + r * 16, lane4 = (odd ? 8u : 2u * (unsigned)DUO) + r * 16 + hh * 4, voff = v_off(head, r, hh);
    const char* qbase = ws + WS_QF + (size_t)((head * 5) >> 1) * DUO + qblk * 4096;
    const char* kbase = ws + WS_KF + (size_t)((head * 5) >> 1) * DUO;
    const char* vbase = ws + WS_VF;
    f32x32 o0 = {}, o1 = {}, o2 = {}, o3 = {};
    const int cc = (lane & 1) ? (head * DH + DH - 1) >> 7 : (head * DH) >> 7, sel = (lane >> 1) & 3;
    unsigned f0 = ((((sel & 1) ? 5 : 0) + cc) * 16 + ((sel & 1) ? kq * 4 : qblk * 2 + (kq >> 1))) * FS, fv = ((10 + cc) * 16 + kq * 4) * FS;
    unsigned fo = ((((lane >> 1) & 1 ? 10 : 5) + cc) * 16 + kq * 4 + ((lane >> 2) & 3)) * FS;
    u32x2 cw = {(unsigned)(uintptr_t)(lds + L_A + kq * 18432 + lane * 16), (unsigned)(uintptr_t)(lds + L_TICK + (lane & 3) * 4)};
    asm volatile("" : "+{v[0:31]}"(o0), "+{v[32:63]}"(o1), "+{v[64:95]}"(o2), "+{v[96:127]}"(o3), "+v"(f0), "+v"(fv), "+v"(fo), "+v"(cw), "+v"(lane8), "+v"(lane4), "+v"(voff));
    publish((gp)ws + WS_PFLAG + pflag * FS, lds + L_SYNC + 12, tag);
    lds_signal(lds + L_SYNC + 20);
    u32x2 kp0, kp1;
    await2(ws + WS_PFLAG, f0, tag, kp0, kp1);
    QK q0, q1, k0, k1; bf16x8 v0, v1;
    const char* kt = kbase + kq * 8192; const char* vt = vbase + (size_t)(kq * 16) * TILE;
    ld_qk(k0, kt, lane8, lane4); ld_qk(q0, qbase + kq * 1024, lane8, lane4); ld_qk(q1, qbase + kq * 1024 + 512, lane8, lane4);
    u32x2 vf;
    ld8_dev(vf, fv, ws + WS_PFLAG); ld_qk(k1, kt + 512, lane8, lane4);
    WAITV(2);
    PIN_QK(k0); PIN_QK(q0); PIN_QK(q1); asm volatile("" : "+v"(kp0), "+v"(kp1));
    u32x32 qa; u32x16 qb;
    {
        const f32x16 s0 = score(k0, q0), s1 = score(k0, q1);
        const u32x2 b0 = {q0.b, hh ? 0u : cvtpk(-max16(s0), 0.f)}, b1 = {q1.b, hh ? 0u : cvtpk(-max16(s1), 0.f)};
        ldsp qs = lds + L_Q + kq * 3072;
        *(LAS bf16x8*)(qs + lane * 16) = q0.a; *(LAS bf16x8*)(qs + 1024 + lane * 16) = q1.a;
        *(LAS u32x4*)(qs + 2048 + lane * 16) = u32x4{b0[0], b0[1], b1[0], b1[1]};
        lds_signal(lds + L_SYNC + 28);
        flag_check<3>(vf, ws + WS_PFLAG, fv, tag);
        ld16_dev(v0, voff, vt); ld16_dev(v1, voff, vt + 1024);
        lds_signal(lds + L_SYNC + 20);
        lds_wait<false>(lds + L_SYNC + 28, 4);
#pragma unroll
        for (int i = 0; i < 4; ++i) {
            ldsp qr = lds + L_Q + ((i + 1 + kq) & 3) * 3072 + lane * 16;
            const u32x4 t0 = *(const LAS u32x4*)qr, t1 = *(const LAS u32x4*)(qr + 1024), t2 = *(const LAS u32x4*)(qr + 2048);
#pragma unroll
            for (int e = 0; e < 4; ++e) { qa[8 * i + e] = t0[e]; qa[8 * i + 4 + e] = t1[e]; qb[4 * i + e] = t2[e]; }
        }
    }
    WAITV(0); lds_signal(lds + L_SYNC + 52); PIN_QK(k1); asm volatile("" : "+v"(v0), "+v"(v1));
    {
        const u32x4 w0 = __builtin_bit_cast(u32x4, vfix(v0, r)), w1 = __builtin_bit_cast(u32x4, vfix(v1, r));
        const u32x4 t0 = __builtin_bit_cast(u32x4, k0.a), t1 = __builtin_bit_cast(u32x4, k1.a);
        u32x32 kv = {t0[0], t0[1], t0[2], t0[3], k0.b, 0x3F80u, t1[0], t1[1], t1[2], t1[3], k1.b, 0x3F80u,
                     w0[0], w0[1], w0[2], w0[3], w1[0], w1[1], w1[2], w1[3], w0[0], w0[1], w0[2], w0[3], w1[0], w1[1], w1[2], w1[3], lane8, lane4, voff, fo};
        const char* fp = ws + WS_PFLAG;
        const char* kp = kt + 1024; const char* vp = vt + TILE;
        asm volatile(R"ASM(
.macro AS X, Y, K, QA, QB, OP, VP, OC, VC, H0, H1, H2, H3, H4, H5, H6, H7, H8, H9, H10, H11, H12, H13, H14, H15
  v_mfma_f32_32x32x16_bf16 v[\Y:\Y+15], v[\K:\K+3], v[\QA:\QA+3], 0
  v_exp_f32 v[\X+0], v[\X+0]
  \H0
  v_exp_f32 v[\X+1], v[\X+1]
  \H1
  v_exp_f32 v[\X+2], v[\X+2]
  \H2
  v_cvt_pk_bf16_f32 v240, v[\X+0], v[\X+1]
  v_exp_f32 v[\X+3], v[\X+3]
  \H3
  v_exp_f32 v[\X+4], v[\X+4]
  \H4
  v_mfma_f32_32x32x8_bf16 v[\Y:\Y+15], v[\K+4:\K+5], v[\QB:\QB+1], v[\Y:\Y+15]
  v_cvt_pk_bf16_f32 v241, v[\X+2], v[\X+3]
  v_exp_f32 v[\X+5], v[\X+5]
  \H5
  v_exp_f32 v[\X+6], v[\X+6]
  \H6
  v_exp_f32 v[\X+7], v[\X+7]
  \H7
  v_cvt_pk_bf16_f32 v242, v[\X+4], v[\X+5]
  v_exp_f32 v[\X+8], v[\X+8]
  \H8
  v_cvt_pk_bf16_f32 v243, v[\X+6], v[\X+7]
  v_exp_f32 v[\X+9], v[\X+9]
  \H9
  v_mfma_f32_32x32x16_bf16 v[\OP:\OP+15], v[\VP+4:\VP+7], v[244:247], v[\OP:\OP+15]
  v_exp_f32 v[\X+10], v[\X+10]
  \H10
  v_exp_f32 v[\X+11], v[\X+11]
  \H11
  v_exp_f32 v[\X+12], v[\X+12]
  \H12
  v_mfma_f32_32x32x16_bf16 v[\OC:\OC+15], v[\VC:\VC+3], v[240:243], v[\OC:\OC+15]
  v_cvt_pk_bf16_f32 v244, v[\X+8], v[\X+9]
  v_cvt_pk_bf16_f32 v245, v[\X+10], v[\X+11]
  v_exp_f32 v[\X+13], v[\X+13]
  \H13
  v_exp_f32 v[\X+14], v[\X+14]
  \H14
  v_exp_f32 v[\X+15], v[\X+15]
  \H15
  v_cvt_pk_bf16_f32 v246, v[\X+12], v[\X+13]
  v_cvt_pk_bf16_f32 v247, v[\X+14], v[\X+15]
.endm
.macro AH0
  s_waitcnt vmcnt(2)
.endm
.macro AH1
  s_mov_b64 exec, s[46:47]
  global_load_dwordx4 v[228:231], v238, s[42:43] sc1
  s_mov_b64 exec, -1
.endm
.macro AH2
  s_mov_b64 exec, s[46:47]
  global_load_dwordx4 v[232:235], v238, s[42:43] offset:1024 sc1
  s_mov_b64 exec, -1
.endm
.macro AH3
  s_waitcnt vmcnt(2)
.endm
.macro AH4
  global_load_dwordx4 v[208:211], v236, s[40:41] offset:0 sc1
.endm
.macro AH5
  global_load_dword v212, v237, s[40:41] offset:0 sc1
.endm
.macro AH6
  s_waitcnt vmcnt(2)
.endm
.macro AH7
  s_cmp_lg_u32 s44, 7
  s_cbranch_scc1 4f
  global_load_dwordx2 v[248:249], v239, s[52:53] sc1
  4:
.endm
.macro AH8
  s_mov_b64 exec, s[46:47]
  global_load_dwordx4 v[220:223], v238, s[48:49] sc1
  s_mov_b64 exec, -1
.endm
.macro AH9
  s_mov_b64 exec, s[46:47]
  global_load_dwordx4 v[224:227], v238, s[48:49] offset:1024 sc1
  s_mov_b64 exec, -1
.endm
.macro AH10
  s_add_u32 s42, s42, 81920
.endm
.macro AH11
  s_addc_u32 s43, s43, 0
.endm
.macro AH12
  s_add_u32 s48, s48, 81920
.endm
.macro AH13
  s_addc_u32 s49, s49, 0
.endm
.macro AH14
  s_waitcnt vmcnt(2)
.endm
.macro AH15
  global_load_dwordx4 v[214:217], v236, s[40:41] offset:512 sc1
.endm
.macro AH16
  global_load_dword v218, v237, s[40:41] offset:512 sc1
.endm
.macro AH17
  s_add_u32 s40, s40, 1024
.endm
.macro AH18
  s_addc_u32 s41, s41, 0
.endm
.macro AH19
  s_sub_u32 s44, s44, 1
.endm
.macro AH20
  s_waitcnt vmcnt(2)
.endm
.macro AH21
  s_mov_b64 exec, s[46:47]
  global_load_dwordx4 v[228:231], v238, s[42:43] sc1
  s_mov_b64 exec, -1
.endm
.macro AH22
  s_mov_b64 exec, s[46:47]
  global_load_dwordx4 v[232:235], v238, s[42:43] offset:1024 sc1
  s_mov_b64 exec, -1
.endm
.macro AH23
  ds_read_b32 v248, v251
.endm
.macro AH24
  s_waitcnt vmcnt(2)
.endm
.macro AH25
  s_waitcnt vmcnt(0)
.endm
.macro AH26
  s_waitcnt lgkmcnt(0)
.endm
.macro AH27
  s_mov_b32 s45, 0x100000
  5:
  v_cmp_gt_u32 vcc, 11, v248
  s_cbranch_vccz 6f
  s_sleep 1
  ds_read_b32 v248, v251
  s_sub_u32 s45, s45, 1
  s_waitcnt lgkmcnt(0)
  s_cbranch_scc0 5b
  6:
.endm
.macro AH28
  ds_write_b128 v250, v[0:3] offset:0
.endm
.macro AH29
  ds_write_b128 v250, v[4:7] offset:1024
.endm
.macro AH30
  ds_write_b128 v250, v[8:11] offset:2048
.endm
.macro AH31
  ds_write_b128 v250, v[16:19] offset:3072
.endm
.macro AH32
  ds_write_b128 v250, v[20:23] offset:4096
.endm
.macro AH33
  ds_write_b128 v250, v[24:27] offset:5120
.endm
.macro AH34
  ds_write_b128 v250, v[32:35] offset:6144
.endm
.macro AH35
  ds_write_b128 v250, v[36:39] offset:7168
.endm
.macro AH36
  ds_write_b128 v250, v[40:43] offset:8192
.endm
.macro AH37
  ds_write_b128 v250, v[48:51] offset:9216
.endm
.macro AH38
  ds_write_b128 v250, v[52:55] offset:10240
.endm
.macro AH39
  ds_write_b128 v250, v[56:59] offset:11264
.endm
.macro AH40
  ds_write_b128 v250, v[64:67] offset:12288
.endm
.macro AH41
  ds_write_b128 v250, v[68:71] offset:13312
.endm
.macro AH42
  ds_write_b128 v250, v[72:75] offset:14336
.endm
.macro AH43
  ds_write_b128 v250, v[80:83] offset:15360
.endm
.macro AH44
  ds_write_b128 v250, v[84:87] offset:16384
.endm
.macro AH45
  ds_write_b128 v250, v[88:91] offset:17408
.endm
  s_setprio 1
  s_nop 0
  v_mfma_f32_32x32x16_bf16 v[128:143], v[208:211], v[160:163], 0
  v_mfma_f32_32x32x8_bf16 v[128:143], v[212:213], v[192:193], v[128:143]
  v_mov_b32 v244, 0
  v_mov_b32 v245, 0
  v_mov_b32 v246, 0
  v_mov_b32 v247, 0
  s_mov_b32 s44, 7
  s_mov_b32 s46, 0x000fffff
  s_mov_b32 s47, 0x000fffff
  s_add_u32 s48, s42, 40960
  s_addc_u32 s49, s43, 0
  s_nop 3
1:
  AS 128, 144, 208, 164, 194, 112, 228, 0, 220, , , , AH0, , , , , , , , , , , , 
  AS 144, 128, 208, 168, 196, 0, 220, 16, 220, , , , , AH1, , AH2, , , , , , , , , 
  AS 128, 144, 208, 172, 198, 16, 220, 32, 220, , , , , , , , , , , , , , , , 
  AS 144, 128, 208, 176, 200, 32, 220, 48, 220, , , , , , , , , , , , , , , , 
  AS 128, 144, 208, 180, 202, 48, 220, 64, 220, , , , , , , , , , , , , , , , 
  AS 144, 128, 208, 184, 204, 64, 220, 80, 220, , , , , , , , , , , , , , , , 
  AS 128, 144, 208, 188, 206, 80, 220, 96, 220, , , , , , , , , , , , , , , AH3, 
  AS 144, 128, 214, 160, 192, 96, 220, 112, 220, , , , AH4, , AH5, , , , , , , , , , 
  AS 128, 144, 214, 164, 194, 112, 220, 0, 228, , , , AH6, , , , , , , , , , , , 
  AS 144, 128, 214, 168, 196, 0, 228, 16, 228, , , , AH7, AH8, , AH9, , , , , , , , , 
  AS 128, 144, 214, 172, 198, 16, 228, 32, 228, , , , , , , , , , , , , , , , 
  AS 144, 128, 214, 176, 200, 32, 228, 48, 228, , , , AH10, AH11, , AH12, AH13, , , , , , , , 
  AS 128, 144, 214, 180, 202, 48, 228, 64, 228, , , , , , , , , , , , , , , , 
  AS 144, 128, 214, 184, 204, 64, 228, 80, 228, , , , , , , , , , , , , , , , 
  AS 128, 144, 214, 188, 206, 80, 228, 96, 228, , , , , , , , , , , , , , , AH14, 
  AS 144, 128, 208, 160, 192, 96, 228, 112, 228, , , , AH15, , AH16, AH17, AH18, , , , , , AH19, , 
  s_cmp_lg_u32 s44, 6
  s_cbranch_scc1 3f
  s_mov_b32 s45, 0x100000
2:
  v_cmp_ne_u64 vcc, s[54:55], v[248:249]
  s_cbranch_vccz 3f
  s_sleep 2
  global_load_dwordx2 v[248:249], v239, s[52:53] sc1
  s_sub_u32 s45, s45, 1
  s_waitcnt vmcnt(0)
  s_cbranch_scc0 2b
3:
  s_cmp_lg_u32 s44, 0
  s_cbranch_scc1 1b
  AS 128, 144, 208, 164, 194, 112, 228, 0, 220, , , , AH20, , , , , , , , , , , , 
  AS 144, 128, 208, 168, 196, 0, 220, 16, 220, , , , , AH21, , AH22, , , , , , , , , 
  AS 128, 144, 208, 172, 198, 16, 220, 32, 220, , , , AH23, , , , , , , , , , , , 
  AS 144, 128, 208, 176, 200, 32, 220, 48, 220, , , , , , , , , , , , , , , , 
  AS 128, 144, 208, 180, 202, 48, 220, 64, 220, , , , , , , , , , , , , , , , 
  AS 144, 128, 208, 184, 204, 64, 220, 80, 220, , , , , , , , , , , , , , , , 
  AS 128, 144, 208, 188, 206, 80, 220, 96, 220, , , , , , , , , , , , , , , AH24, 
  AS 144, 128, 214, 160, 192, 96, 220, 112, 220, , , , , , , , , , , , , , , , 
  AS 128, 144, 214, 164, 194, 112, 220, 0, 228, , , , AH25, , , , , , , , , , , , 
  AS 144, 128, 214, 168, 196, 0, 228, 16, 228, , , , AH26, AH27, , , , , , , , , , , 
  AS 128, 144, 214, 172, 198, 16, 228, 32, 228, , , , AH28, , , AH29, , , , , , , AH30, , 
  AS 144, 128, 214, 176, 200, 32, 228, 48, 228, , , , AH31, , , AH32, , , , , , , AH33, , 
  AS 128, 144, 214, 180, 202, 48, 228, 64, 228, , , , AH34, , , AH35, , , , , , , AH36, , 
  AS 144, 128, 214, 184, 204, 64, 228, 80, 228, , , , AH37, , , AH38, , , , , , , AH39, , 
  AS 128, 144, 214, 188, 206, 80, 228, 96, 228, , , , AH40, , , AH41, , , , , , , AH42, , 
  AS 144, 128, 208, 160, 192, 96, 228, 112, 228, , , , AH43, , , AH44, , , , , , , AH45, , 
  v_mov_b32 v248, 1
  s_setprio 3
  v_mfma_f32_32x32x16_bf16 v[112:127], v[232:235], v[244:247], v[112:127]
  s_waitcnt lgkmcnt(0)
  s_mov_b64 exec, 1
  ds_add_u32 v251, v248 offset:16
  s_mov_b64 exec, -1
  s_nop 7
.purgem AS
.purgem AH0
.purgem AH1
.purgem AH2
.purgem AH3
.purgem AH4
.purgem AH5
.purgem AH6
.purgem AH7
.purgem AH8
.purgem AH9
.purgem AH10
.purgem AH11
.purgem AH12
.purgem AH13
.purgem AH14
.purgem AH15
.purgem AH16
.purgem AH17
.purgem AH18
.purgem AH19
.purgem AH20
.purgem AH21
.purgem AH22
.purgem AH23
.purgem AH24
.purgem AH25
.purgem AH26
.purgem AH27
.purgem AH28
.purgem AH29
.purgem AH30
.purgem AH31
.purgem AH32
.purgem AH33
.purgem AH34
.purgem AH35
.purgem AH36
.purgem AH37
.purgem AH38
.purgem AH39
.purgem AH40
.purgem AH41
.purgem AH42
.purgem AH43
.purgem AH44
.purgem AH45
)ASM"
            : "+{v[0:31]}"(o0), "+{v[32:63]}"(o1), "+{v[64:95]}"(o2), "+{v[96:127]}"(o3), "+{v[208:239]}"(kv), "+{s[40:41]}"(kp), "+{s[42:43]}"(vp)
            : "{v[160:191]}"(qa), "{v[192:207]}"(qb), "{v[250:251]}"(cw), "{s[52:53]}"(fp), "{s[54:55]}"(tag)
            : "v128", "v129", "v130", "v131", "v132", "v133", "v134", "v135", "v136", "v137", "v138", "v139", "v140", "v141", "v142", "v143",
              "v144", "v145", "v146", "v147", "v148", "v149", "v150", "v151", "v152", "v153", "v154", "v155", "v156", "v157", "v158", "v159",
              "v240", "v241", "v242", "v243", "v244", "v245", "v246", "v247", "v248", "v249",
              "s44", "s45", "s46", "s47", "s48", "s49", "vcc", "scc", "memory");
    }
    lane = lane_id(); r = lane & 31; hh = lane >> 5;
    lane8 = (hh + odd) * (unsigned)DUO + r * 16; lane4 = (odd ? 8u : 2u * (unsigned)DUO) + r * 16 + hh * 4; voff = v_off(head, r, hh);
    lds_wait<false>(lds + L_TICK + 16, 4);
    f32x4 own[6];
#pragma unroll
    for (int g = 0; g < 6; ++g) {
        ldsp sp = lds + L_A + g * 1024 + lane * 16;
        own[g] = (quad(o3, (g / 3) * 16 + (g % 3) * 4) + *(const LAS f32x4*)(sp + ((kq - 1) & 3) * 18432)) +
                 (*(const LAS f32x4*)(sp + ((kq - 2) & 3) * 18432 + 6144) + *(const LAS f32x4*)(sp + ((kq - 3) & 3) * 18432 + 12288));
    }
    auto store_o = [&]() __attribute__((always_inline)) {
        const int lh = head & 7;
        char* ob = ws + WS_OF + (size_t)((qblk * 8 + 2 * kq) * 40 + (head >> 3) * 10) * 1024;
        u32x2 last[2];
#pragma unroll
        for (int i = 0; i < 2; ++i) {
            const float rl = __builtin_amdgcn_rcpf(halves(own[3 * i + 2][0])[1]);
            const f32x4 w0 = own[3 * i] * rl, w1 = own[3 * i + 1] * rl, w2 = own[3 * i + 2] * rl;
            st16_dev(lane * 16, ob + (i * 40 + lh) * 1024,
                     __builtin_bit_cast(bf16x8, u32x4{cvtpk(w0[0], w0[1]), cvtpk(w0[2], w0[3]), cvtpk(w1[0], w1[1]), cvtpk(w1[2], w1[3])}));
            last[i] = u32x2{cvtpk(w2[0], w2[1]), cvtpk(w2[2], w2[3])};
        }
        const u32x2 lv = {__builtin_amdgcn_permlane32_swap(last[0][0], last[1][0], false, false)[0], __builtin_amdgcn_permlane32_swap(last[0][1], last[1][1], false, false)[0]};
        st8_dev(hh * (40 * 1024) + r * 8, ob + (8 + (lh >> 2)) * 1024 + (lh & 3) * 256, lv);
    };
    store_o();
    float chk = 0.f;
#pragma unroll
    for (int g = 0; g < 6; ++g) chk += (fabsf(own[g][0]) + fabsf(own[g][1])) + (fabsf(own[g][2]) + fabsf(own[g][3]));
    if (__builtin_expect(__any(!(chk < 1e30f) || (hh && !(fminf(own[2][0], own[5][0]) > 1e-30f))), 0)) {
        const int c0 = (head * DH) >> 7, c1 = (head * DH + DH - 1) >> 7;
        await((gcp)ws + WS_PFLAG + (((lane & 32) ? 10 : 5) + ((lane & 16) ? c1 : c0)) * (16 * FS) + (lane & 15) * FS, tag);
#pragma unroll
        for (int i = 0; i < 2; ++i) {
            QK q1; ld_qk(q1, qbase + (2 * kq + i) * 512, lane8, lane4); WAITV(0); PIN_QK(q1);
            f32x16 oo = {}; float m = -INFINITY;
#pragma unroll 1
            for (int t = 0; t < 64; ++t) {
                QK kf; bf16x8 va, vb;
                ld_qk(kf, kbase + t * 512, lane8, lane4); ld16_dev(va, voff, vbase + (size_t)t * TILE); ld16_dev(vb, voff, vbase + (size_t)t * TILE + 1024);
                WAITV(0); PIN_QK(kf); asm volatile("" : "+v"(va), "+v"(vb));
                f32x16 s = score(kf, q1);
                const f32x2 mh = halves(max16(s));
                const float mn = fmaxf(m, fmaxf(mh[0], mh[1])), al = __builtin_amdgcn_exp2f(m - mn);
                m = mn;
#pragma unroll
                for (int e = 0; e < 16; ++e) { s[e] = __builtin_amdgcn_exp2f(s[e] - mn); oo[e] *= al; }
                oo = MFMA32(vfix(va, r), pack8(s, 0), oo); oo = MFMA32(vfix(vb, r), pack8(s, 1), oo);
            }
#pragma unroll
            for (int g = 0; g < 3; ++g) own[3 * i + g] = quad(oo, 4 * g);
        }
        WAITV(0);
        store_o();
    }
    publish((gp)ws + WS_OFLAG + (qblk * 32 + head) * FS, lds + L_SYNC + 4, tag);
}

constexpr int gu_w(int s) { return (s & 3) == 0 ? 65536 : (s & 3) == 1 ? 90112 : (s & 3) == 2 ? 0 : 24576; }
constexpr int gu_x(int s) { return s % 3 == 0 ? 131072 : s % 3 == 1 ? 114688 : 49152; }

constexpr int dn_slot(int s) { return s < 10 ? s : s < 16 ? 8 + (s & 1) : s < 26 ? s - 16 : s - 26; }
constexpr int dn_bstep(int s) { return s >= 1 && s <= 16 ? 16 + (s - 1) / 2 : s + 7; }
constexpr int dn_bfirst(int s) { return s >= 1 && s <= 16 && (s & 1) == 0 ? 2 : 0; }
constexpr int dn_bcount(int s) { return s == 0 || s >= 19 ? 0 : s <= 16 ? ((s & 1) ? 2 : 1) : 3; }
constexpr int dn_younger(int s) {
    int n = s < 5 ? 2 * (4 - s) : 0;
    for (int t = (s > 4 ? s - 4 : 0); t < s; ++t) n += dn_bcount(t) + (t + 5 < 24 ? 2 : 0);
    return n;
}
DI void gwave_body(const char* x, const char* w, const char* wo, const char* wg, const char* wu, const char* wd, char* ws, char* out, ldsp lds,
                   int rp, int cp, int rb, int cb, int mb, int nb, int gw) {
    const int lane = lane_id(), r = lane & 31, hh = lane >> 5;
    const unsigned lds0 = (unsigned)(uintptr_t)lds;
    const unsigned voffA0 = (lane >> 3) * (D * 2) + (((lane & 7) ^ ((lane >> 4) & 7)) << 4);
    const unsigned voffA1 = (lane >> 3) * (D * 2) + (((lane & 7) ^ ((4 + (lane >> 4)) & 7)) << 4);
    const unsigned voffB = (lane >> 4) * (3 * D * 2) + (((lane & 15) ^ ((lane >> 4) << 2)) << 4);
    const u32x4 srdX = make_srd(x, S * D * 2), srdW = make_srd(w, D * 3 * D * 2), srdG = make_srd(wg, D * FF * 2), srdU = make_srd(wu, D * FF * 2);
    const u32x4 srdS = make_srd(ws, (unsigned)WS_END);
    const unsigned baseA = (rb * 128 + gw * 32) * (D * 2), baseP = (rp * 128 + gw * 32) * (D * 2), baseB = gw * 16 * (3 * D * 2) + cp * 256, baseG = gw * 16 * (FF * 2) + cb * 192;
    const unsigned ld4k = lds0 + gw * 4096, ld3k = lds0 + gw * 3072;
    const int rot = ((rp + cp) & 1) * 5;
    auto ksl = [&](int s) { return s + rot - (s + rot >= 10 ? 10 : 0); };
    unsigned voffW[3];
#pragma unroll
    for (int i = 0; i < 3; ++i) { const int ch = i * 64 + lane, row = ch / 12; voffW[i] = row * (FF * 2) + (ch - row * 12) * 16; }
    auto dmaP = [&](auto sc, auto ic) __attribute__((always_inline)) {
        constexpr int s = decltype(sc)::v, i = decltype(ic)::v, slot = (s % P_NS) * P_SLOT;
        if constexpr (i < 4) dma16<i * 8 * (D * 2), slot + i * 1024>((i & 1) ? voffA1 : voffA0, srdX, baseP + ksl(s) * 128, ld4k);
        else dma16<(i - 4) * 4 * (3 * D * 2), slot + 16384 + (i - 4) * 1024>(voffB, srdW, baseB + ksl(s) * (64 * 3 * D * 2), ld4k);
    };
    auto dmaG = [&](auto sc, auto ic) __attribute__((always_inline)) {
        constexpr int s = decltype(sc)::v, i = decltype(ic)::v;
        if constexpr (i < 4) dma16<(int)WS_XN + i * 8 * (D * 2), gu_x(s) + i * 1024, true>((i & 1) ? voffA1 : voffA0, srdS, baseA + s * 128, ld4k);
        else dma16<0, gu_w(s) + (i < 7 ? 0 : 12288) + ((i - 4) % 3) * 1024>(voffW[(i - 4) % 3], i < 7 ? srdG : srdU, baseG + s * (64 * FF * 2), ld3k);
    };
    const bool proj = cp < 15;
    const int rank = 255 - (int)blockIdx.x, ru = rank * 4 + gw;
    const bool ret = rank < 170;
    u32x2 est = {0u, 0u};
    PRIO(2);
    ldsp pc[2];
    const bf16x8 sel = colsel(lane);
    f32x4 cs = {};
    auto pstage = [&](auto sc) __attribute__((always_inline)) {
        constexpr int s = decltype(sc)::v;
        if (s < 2) WAITV(16); else if (s == 7) WAITV(9); else if (s < 9) WAITV(8); else WAITV(10);
        HWBAR();
        bf16x8 cf[4];
#pragma unroll
        for (int k = 0; k < 4; ++k) cf[k] = cat(ldstr(pc[(s >> 1) & 1] + (s & 1) * P_SLOT + k * 4096), ldstr(pc[(s >> 1) & 1] + (s & 1) * P_SLOT + k * 4096 + 1024));
        SBAR();
        sfor<0, 8>([&](auto ic) __attribute__((always_inline)) {
            constexpr int i = decltype(ic)::v;
            if constexpr (s != 1 && s + 2 < 10) dmaP(IC<(s == 0 ? 3 : s + 2)>{}, ic);
            if (i & 1) cs = MFMA16(sel, cf[i >> 1], cs);
            SBAR();
        });
        if (s == 6) ld8_dev(est, r * 64, ws + WS_STAT + (rb * 128 + gw * 32) * 64);
    };
    if (proj) {
        sfor<0, 24>([&](auto ic) __attribute__((always_inline)) { dmaP(IC<decltype(ic)::v / 8>{}, IC<decltype(ic)::v % 8>{});
        });
        pc[0] = lds + 16384 + (8 * hh + ((lane & 15) >> 2)) * 256 + (((4 * gw + 2 * ((lane >> 4) & 1) + ((lane & 3) >> 1)) ^ (lane & 12)) << 4) + 8 * (lane & 1); pc[1] = pc[0] + 65536;
        asm volatile("" : "+v"(pc[0]), "+v"(pc[1]));
        sfor<0, 9>(pstage);
        asm volatile("" : "+v"(est));
    } else HWBAR();
    const u32x2 tg = *(const LAS u32x2*)(lds + L_SYNC + 56);
    const u64 tag = ((u64)(unsigned)__builtin_amdgcn_readfirstlane(tg[1]) << 32) | (unsigned)__builtin_amdgcn_readfirstlane(tg[0]);
    if (!proj) est[1] = ~(unsigned)tag;
    (void)load_stat(ws, rb * 128 + gw * 32 + r, tag, est);
    sfor<0, 10>([&](auto ic) __attribute__((always_inline)) { dmaG(IC<0>{}, ic); });
    if (proj) {
        pstage(IC<9>{});
        *(LAS bf16x4*)(lds + L_CS + (gw * 64 + lane) * 8) = colsum_op(hh ? 0.f : cs[0]);
        lds_signal(lds + L_SYNC + 16);
    }
    PRIO(0);
    ldsp tp = lds + L_TICK + (lane & 3) * 4;
    unsigned one = 1u; asm volatile("" : "+v"(one));
    const u64 tmask = 1ull << gw;
    const int half = nb & 1, rt0 = mb * 2 + half, rt1 = mb * 2 + (half ^ 1), ct0 = (nb >> 1) * 5 + half * 4, cstep = half ? -1 : 1;
    const unsigned voffF = lane * 16;
    unsigned bB[3];
#pragma unroll
    for (int i = 0; i < 3; ++i) bB[i] = (unsigned)WS_WF + (ct0 + i * cstep) * 1024;
    const int pflip = (mb + (nb >> 1)) & 3, srot = half * 3;
    auto ks2 = [&](int s) { return (gw + 4 * ((s / 6) ^ pflip)) * 6 + (s % 6 < 3 ? s % 6 + srot : s % 6 - srot); };
    auto kofB = [&](int s) { return (unsigned)(s < 24 ? ks2(s) : 96 + gw * 10 + (s - 24)) * (20 * 1024); };
    bf16x8 fa[5][2], fb[10][3];
    auto ldB = [&](int s, int i) { bld16_dev(fb[dn_slot(s)][i], voffF, srdS, bB[i], kofB(s)); };
    u32x2 hf;
    const unsigned rfo = (hh ? min((480 + gw * 50) / 4 + r, (529 + gw * 50) / 4) : ((gw + (r >> 3) * 4) * 30) / 4 + (r & 7)) * FS;
    {
        const int blk = (lane >> 4) & 1, q = (lane & 15) >> 2, p = lane & 3;
        ldsp pb[2], pg[2][4];
        pb[0] = lds + (8 * hh + q) * 192 + blk * 32 + p * 8; pb[1] = pb[0] + 65536;
        asm volatile("" : "+v"(pb[0]), "+v"(pb[1]));
#pragma unroll
        for (int k = 0; k < 4; ++k) {
            pg[0][k] = lds + gw * 4096 + r * 128 + (((2 * k + hh) ^ ((r >> 1) & 7)) << 4); pg[1][k] = pg[0][k] + 114688;
            asm volatile("" : "+v"(pg[0][k]), "+v"(pg[1][k]));
        }
        f32x16 acc[6] = {};
        bf16x8 b[4], a[2];
        constexpr int LA = 3;
        WAITV(0);
        tick(tp, one, tmask);
        unsigned tk = 0;
        sfor<0, 240 + LA>([&](auto jc) __attribute__((always_inline)) {
            constexpr int j = decltype(jc)::v;
            if (j < 240) {
                constexpr int s = j / 24, k = (j % 24) / 6, c = j % 6;
                if (j % 24 == 20) tk = tick_peek(tp);
                if (j == 12) {
                    lds_wait(lds + L_SYNC, 4);
                    sfor<0, 10>([&](auto ic) __attribute__((always_inline)) { dmaG(IC<1>{}, ic); });
                }
                if (j == 24) { WAITV(0); tick(tp, one, tmask); }
                if (j == 48) {
                    PRIO(2);
                    lds_wait(lds + L_SYNC + 20, 8);
                    PRIO(0);
                    sfor<0, 20>([&](auto ic) __attribute__((always_inline)) { dmaG(IC<2 + decltype(ic)::v / 10>{}, IC<decltype(ic)::v % 10>{}); });
                    asm volatile("s_waitcnt lgkmcnt(0)" ::: "memory");
                    if (ret) retile_fetch(wd, wo, lds0 + gu_x(1) + gw * 4096, ru, lane);
                    WAITV(0);
                    PRIO(2);
                    if (ret) retile_store(ws, lds + gu_x(1) + gw * 4096, ru, lane);
                    tick(tp, one, tmask);
                }
                if (j % 24 == 0) tick_wait<0>(tk, tp, s + 1);
                constexpr int xh = gu_x(s) >= 114688, wh = gu_w(s) >= 65536, wo_ = gu_w(s) - wh * 65536 + (c / 3) * 12288 + (c % 3) * 64 + k * 3072;
                if (c == 0) a[k & 1] = lds128(pg[xh][k] + (gu_x(s) - xh * 114688));
                b[j & 3] = cat(ldstr(pb[wh] + wo_), ldstr(pb[wh] + wo_ + 768));
            }
            SBAR();
            if (j >= LA) {
                constexpr int m = j >= LA ? j - LA : 0, s = m / 24, i = m % 24;
                acc[m % 6] = MFMA32(b[m & 3], a[(i / 6) & 1], acc[m % 6]);
                SBAR();
                if (i == 0 && s == 4) { PRIO(2); publish((gp)ws + WS_RFLAG + rank * FS, lds + L_SYNC + 24, tag); PRIO(0); }
                if (i == 8 && s >= 2 && s < 9) { WAITV(4); tick(tp, one, tmask); }
                if constexpr (i % 2 == 0 && i < 20 && s >= 2 && s + 2 < 10) dmaG(IC<s + 2>{}, IC<i / 2>{});
                if (i == 21 && s == 6) ld8_dev(hf, rfo, ws + WS_RFLAG);
                if (i == 22 && s == 7) flag_check<0>(hf, ws + WS_RFLAG, rfo, tag);
                if constexpr (i % 2 == 0 && s >= 8) ldB(((s - 8) * 12 + i / 2) / 3, ((s - 8) * 12 + i / 2) % 3);
            }
            SBAR();
        });
        PRIO(2);
        tick(tp, one, tmask);
        char* hb = ws + WS_HF + (size_t)((rb * 4 + gw) * 96 + cb * 6) * 1024;
#pragma unroll
        for (int c = 0; c < 3; ++c) {
            f32x16 h;
#pragma unroll
            for (int e = 0; e < 16; ++e) h[e] = fmaxf(acc[c][e], 0.f) * acc[c + 3][e];
            st16_dev(voffF, hb + (2 * c) * 1024, pack8(h, 0));
            st16_dev(voffF, hb + (2 * c + 1) * 1024, pack8(h, 1));
        }
        publish((gp)ws + WS_HFLAG + (rb * 16 + cb) * FS, lds + L_SYNC + 8, tag);
#pragma unroll
        for (int i = 0; i < 8; ++i) asm volatile("" : "+v"(fb[i][0]), "+v"(fb[i][1]), "+v"(fb[i][2]));
    }
    if (blockIdx.x == 255 && gw == 0) {
#pragma unroll 1
        for (int i = 0; i < 4; ++i) await((gcp)ws + WS_PFLAG + (i * 64 + lane_id()) * FS, tag);
        if (lane_id() == 0) __hip_atomic_store((GAS u64*)((gp)ws + WS_EPOCH), tag, RLX_AGENT);
    }
    const unsigned bH0 = (unsigned)WS_HF + rt0 * 96 * 1024, bH1 = (unsigned)WS_HF + rt1 * 96 * 1024;
    const unsigned bO0 = (unsigned)WS_OF + (rt0 * 40 + gw * 10) * 1024, bO1 = (unsigned)WS_OF + (rt1 * 40 + gw * 10) * 1024;
    auto ldA = [&](int s, int i) { bld16_dev(fa[s % 5][i], voffF, srdS, i ? bH1 : bH0, ks2(s) * 1024); };
    const char* hfl = ws + WS_HFLAG + ((mb >> 1) * 16 + gw) * FS;
    auto hflag = [&](int p) { return hfl + (p ^ pflip) * (4 * FS); };
    unsigned zero = 0u; asm volatile("" : "+v"(zero));
    PRIO(0);
    ld8_dev(hf, zero, hflag(0));
#pragma unroll
    for (int i = 0; i < 6; ++i) { ldB(8 + i / 3, i % 3); __builtin_amdgcn_s_sleep(2); }
    tick_wait<2>(0u, tp, 11);
    lds_wait(lds + L_SYNC + 52, 4);
    ldsp my = lds + L_G + gw * 20480 + lane * 16;
    PRIO(0);
    sfor<0, 18>([&](auto ic) __attribute__((always_inline)) {
        constexpr int i = decltype(ic)::v;
        dma16<0, i * 1024, true>(voffF, srdS, bB[i % 3] + kofB(10 + i / 3), lds0 + L_G + gw * 20480); __builtin_amdgcn_s_sleep(2);
    });
    WAITV(24);
    flag_check<0>(hf, hflag(0), zero, tag);
    ld8_dev(hf, zero, hflag(1));
#pragma unroll
    for (int i = 0; i < 10; ++i) { ldA(i / 2, i % 2); if (i >= 2) __builtin_amdgcn_s_sleep(2); }
    f32x16 acc[5] = {};
    auto stB = [&](auto ic) __attribute__((always_inline)) {
        constexpr int i = decltype(ic)::v;
        dma16<0, i * 1024, true>(voffF, srdS, bB[i % 3] + kofB(29 + i / 3), lds0 + L_G + gw * 20480);
    };
    sfor<0, 24>([&](auto sc) __attribute__((always_inline)) {
        constexpr int s = decltype(sc)::v, wa = s % 5, wb = dn_slot(s);
        if (s == 19) {
#pragma unroll
            for (int i = 0; i < 9; ++i) { ldB(26 + i / 3, i % 3); __builtin_amdgcn_s_sleep(1); }
            sfor<0, 15>([&](auto ic) __attribute__((always_inline)) { stB(ic); __builtin_amdgcn_s_sleep(1); });
            tick_wait<0>(0u, lds + L_TICK + 16, 1);
        }
        asm volatile("s_waitcnt vmcnt(%5)" : "+v"(fa[wa][0]), "+v"(fa[wa][1]), "+v"(fb[wb][0]), "+v"(fb[wb][1]), "+v"(fb[wb][2]) : "i"(dn_younger(s) + (s >= 19 ? 24 : 0)) : "memory");
        if (s == 1 || s == 7 || s == 13) flag_check<0>(hf, hflag((s + 5) / 6), zero, tag);
        if (s == 1 || s == 7) ld8_dev(hf, zero, hflag((s + 5) / 6 + 1));
        if (s >= 9 && s < 15) {
#pragma unroll
            for (int i = 0; i < 3; ++i) fb[dn_slot(s + 1)][i] = lds128(my + ((s - 9) * 3 + i) * 1024);
        }
        SBAR();
        constexpr int bs = dn_bstep(s), b0 = dn_bfirst(s), bn = dn_bcount(s);
        constexpr bool na_ = s + 5 < 24;
        acc[2] = MFMA32(fb[wb][2], fa[wa][0], acc[2]); SBAR(); if (bn > 0) ldB(bs, b0); SBAR();
        acc[0] = MFMA32(fb[wb][0], fa[wa][0], acc[0]); SBAR(); if (bn > 1) ldB(bs, b0 + 1); SBAR();
        acc[1] = MFMA32(fb[wb][1], fa[wa][0], acc[1]); SBAR(); if (bn > 2) ldB(bs, b0 + 2); SBAR();
        acc[3] = MFMA32(fb[wb][0], fa[wa][1], acc[3]); SBAR(); if (na_) ldA(s + 5, 0); SBAR();
        acc[4] = MFMA32(fb[wb][1], fa[wa][1], acc[4]); SBAR(); if (na_) ldA(s + 5, 1); SBAR();
        PIN5(acc);
    });
    PRIO(2);
    const int l3 = lane_id();
    u32x2 xr[5], kp0, kp1;
#pragma unroll
    for (int t = 0; t < 5; ++t)
        ld8_dev(xr[t], (l3 & 31) * (D * 2) + (l3 >> 5) * 8, x + (t < 3 ? rt0 : rt1) * 32 * (D * 2) + ((ct0 + (t < 3 ? t : t - 3) * cstep) * 32 + gw * 8) * 2);
    await2(ws + WS_OFLAG + ((mb >> 2) * 32 + gw * 8) * FS, (l3 & 7) * FS, tag, kp0, kp1);
    bf16x8 fo[10][2], fw[5][3];
    u32x2 fl[2][2][2];
#pragma unroll
    for (int k = 0; k < 8; ++k) { bld16_dev(fo[k][0], voffF, srdS, bO0, k * 1024); bld16_dev(fo[k][1], voffF, srdS, bO1, k * 1024); }
#pragma unroll
    for (int k = 0; k < 8; ++k) ld8_dev(fl[k >> 2][(k >> 1) & 1][k & 1], voffF >> 1, ws + (((k >> 1) & 1) ? bO1 : bO0) + (8 + (k >> 2)) * 1024 + (k & 1) * 512);
    asm volatile("" : "+v"(xr[0]), "+v"(xr[1]), "+v"(xr[2]), "+v"(xr[3]), "+v"(xr[4]));
    sfor<0, 8>([&](auto kc) __attribute__((always_inline)) {
        constexpr int k = decltype(kc)::v, wb = dn_slot(24 + (k < 5 ? k : 0));
        if (k < 5) asm volatile("s_waitcnt vmcnt(%5)" : "+v"(fo[k][0]), "+v"(fo[k][1]), "+v"(fb[wb][0]), "+v"(fb[wb][1]), "+v"(fb[wb][2]) : "i"(22 - 2 * k) : "memory");
        else asm volatile("s_waitcnt vmcnt(%2)" : "+v"(fo[k][0]), "+v"(fo[k][1]) : "i"(22 - 2 * k) : "memory");
        if (k == 0) asm volatile("" : "+v"(kp0), "+v"(kp1));
        if constexpr (k >= 3) {
#pragma unroll
            for (int i = 0; i < 3; ++i) fw[k - 3][i] = lds128(my + ((k - 3) * 3 + i) * 1024);
        }
        SBAR();
        const bf16x8 b0 = k < 5 ? fb[wb][0] : fw[k < 5 ? 0 : k - 5][0], b1 = k < 5 ? fb[wb][1] : fw[k < 5 ? 0 : k - 5][1], b2 = k < 5 ? fb[wb][2] : fw[k < 5 ? 0 : k - 5][2];
        acc[2] = MFMA32(b2, fo[k][0], acc[2]); acc[0] = MFMA32(b0, fo[k][0], acc[0]); acc[1] = MFMA32(b1, fo[k][0], acc[1]);
        acc[3] = MFMA32(b0, fo[k][1], acc[3]); acc[4] = MFMA32(b1, fo[k][1], acc[4]);
        PIN5(acc);
        SBAR();
    });
    asm volatile("s_waitcnt vmcnt(0) lgkmcnt(0)" : "+v"(fl[0][0][0]), "+v"(fl[0][0][1]), "+v"(fl[0][1][0]), "+v"(fl[0][1][1]), "+v"(fl[1][0][0]), "+v"(fl[1][0][1]), "+v"(fl[1][1][0]), "+v"(fl[1][1][1]),
                 "+v"(fw[3][0]), "+v"(fw[3][1]), "+v"(fw[3][2]), "+v"(fw[4][0]), "+v"(fw[4][1]), "+v"(fw[4][2]) :: "memory");
#pragma unroll
    for (int k = 0; k < 4; ++k) fo[8 + (k >> 1)][k & 1] = __builtin_bit_cast(bf16x8, u32x4{fl[k >> 1][k & 1][0][0], fl[k >> 1][k & 1][0][1], fl[k >> 1][k & 1][1][0], fl[k >> 1][k & 1][1][1]});
    sfor<0, 6>([&](auto tc) __attribute__((always_inline)) {
        constexpr int t = decltype(tc)::v;
        if constexpr (t < 5) { acc[t] = MFMA32(fw[3][t % 3], fo[8][t / 3], acc[t]); acc[t] = MFMA32(fw[4][t % 3], fo[9][t / 3], acc[t]); }
        SBAR();
        if constexpr (t >= 1) {
#pragma unroll
            for (int g = 0; g < 4; ++g) *(LAS f32x4*)(my + g * 5120 + (t - 1) * 1024) = quad(acc[t >= 1 ? t - 1 : 0], 4 * g);
        }
        SBAR();
    });
    asm volatile("s_waitcnt lgkmcnt(0)" ::: "memory");
    asm volatile(".Lfb_late:");
    ldsp tp3 = lds + L_TICK + (l3 & 3) * 4;
    tick(tp3, 1u, tmask);
    tick_wait<2>(0u, tp3, 12);
#pragma unroll
    for (int t = 0; t < 5; ++t) {
        ldsp sp = lds + L_G + gw * 5120 + t * 1024 + l3 * 16;
        const f32x4 own = (*(const LAS f32x4*)(sp) + *(const LAS f32x4*)(sp + 20480)) + (*(const LAS f32x4*)(sp + 40960) + *(const LAS f32x4*)(sp + 61440));
        gp op = (gp)out + ((t < 3 ? rt0 : rt1) * 32 + (l3 & 31)) * (D * 2) + ((ct0 + (t < 3 ? t : t - 3) * cstep) * 32 + gw * 8 + (l3 >> 5) * 4) * 2;
        const u32x2 ov = {cvtpk(own[0] + bf_lo(xr[t][0]), own[1] + bf_hi(xr[t][0])), cvtpk(own[2] + bf_lo(xr[t][1]), own[3] + bf_hi(xr[t][1]))};
        *(GAS u32x2*)op = ov;
    }
}

__global__ __launch_bounds__(512, 2) void fused_block(const char* __restrict__ x, const char* __restrict__ wqkv, const char* __restrict__ wo, const char* __restrict__ wg,
                                                      const char* __restrict__ wu, const char* __restrict__ wd, char* __restrict__ ws, char* __restrict__ out) {
    extern __shared__ __attribute__((aligned(16))) char smem[];
    ldsp lds = (ldsp)smem;
    const int bid = blockIdx.x, xcd = bid & 7, idx = bid >> 3;
    const int rb = (xcd >> 1) * 4 + (idx & 3), cb = (xcd & 1) * 8 + (idx >> 2);
    const int tp_ = (xcd & 1) * 30 + idx;
    const int rp = (xcd >> 1) * 4 + (idx < 30 ? tp_ & 3 : (xcd & 1) * 2 + (idx & 1)), cp = idx < 30 ? tp_ >> 2 : 15;
    const int hm = xcd * 4 + (idx >> 3), qn = idx & 7;
    const int wv = __builtin_amdgcn_readfirstlane(threadIdx.x >> 6);
    if (wv >= 4) {
        gwave_body(x, wqkv, wo, wg, wu, wd, ws, out, lds, rp, cp, rb, cb, hm, qn, wv - 4);
    } else {
        PRIO(1);
        if (wv == 3) {
            u64 pc; int len;
            asm volatile("s_getpc_b64 %0\n.Lfb_here:\n\ts_mov_b32 %1, .Lfb_late-.Lfb_here" : "=s"(pc), "=s"(len));
            const int off = max(min((idx * 12 + min(lane_id(), 11)) * 128, len), 0);
            asm volatile("s_mov_b32 m0, %2\n\ts_nop 0\n\tglobal_load_lds_dword %0, %1" ::"v"(off), "s"(pc), "s"((unsigned)(uintptr_t)lds + L_Q + 3 * 3072) : "memory");
        }
        u32x32 pv; f32x32 accA, accB;
        const int srow = rb * 128 + cb * 8 + wv * 2;
        const u64 tag = awave_start(x, ws, lds, srow, wv, pv, accA, accB);
        if (wv == 0) { const int l = lane_id(); if (l < 16) *(LAS unsigned*)(lds + L_SYNC + l * 4) = l == 14 ? (unsigned)tag : l == 15 ? (unsigned)(tag >> 32) : 0u; }
        proj_body(ws, lds, rp, cp, srow, wv, tag, pv, accA, accB);
        attn_body(ws, lds, hm, qn, wv, tag, cp * 16 + rp);
    }
}

extern "C" void kernel_launch(void* const* d_in, const int* in_sizes, int n_in, void* d_out, int out_size, void* d_ws, size_t ws_size,
                              hipStream_t stream) {
    (void)hipFuncSetAttribute((const void*)fused_block, hipFuncAttributeMaxDynamicSharedMemorySize, L_TOTAL);
    fused_block<<<256, 512, L_TOTAL, stream>>>((const char*)d_in[0], (const char*)d_in[1], (const char*)d_in[2], (const char*)d_in[4], (const char*)d_in[5],
                                               (const char*)d_in[6], (char*)d_ws, (char*)d_out);
}
```
